```python
import torch

def _softmax(x, axis=-1):
    return torch.softmax(x, dim=axis)

def _gelu(x):
    return torch.nn.functional.gelu(x, approximate='tanh')

def _silu(x):
    return torch.nn.functional.silu(x)

def _sigmoid(x):
    return torch.sigmoid(x)

def _layernorm(x, axis=-1, eps=1e-05):
    m = x.mean(dim=axis, keepdim=True)
    v = x.var(dim=axis, keepdim=True, unbiased=False)
    return (x - m) / torch.sqrt(v + eps)

def _scatter_add(tgt, idx, src, axis=0):
    return tgt.clone().index_add_(axis, idx, src)

def _segment_sum(src, seg, n):
    out = torch.zeros((n,) + src.shape[1:], dtype=src.dtype)
    return out.index_add_(0, seg, src)

def _pad(x, pw):
    flat = tuple((v for pair in reversed(pw) for v in pair))
    return torch.nn.functional.pad(x, flat)
kernel_name = 'parallel_sum__mha_block__mixer_gmlp__prompt'

def setup_inputs():

    _w = lambda a: (lambda t: t.to(torch.bfloat16) if t.is_floating_point() else t)(torch.as_tensor(a))

    def _A_setup():
        B, S, D, H = (1, 2048, 512, 32)
        return {'x': _w((lambda *s: torch.randn(*s, dtype=torch.bfloat16) * 0.05)(B, S, D)), 'wqkv': _w((lambda *s: torch.randn(*s, dtype=torch.bfloat16) * 0.2)(D, 3 * D)), 'wo': _w((lambda *s: torch.randn(*s, dtype=torch.bfloat16) * 0.2)(D, D))}
    _A_in = _A_setup()

    def _B_setup():
        B, S, D, F = (1, 2048, 512, 1792)
        Fh = F // 2
        return {'x': _w((lambda *s: torch.randn(*s, dtype=torch.bfloat16) * 0.05)(B, S, D)), 'w_in': _w((lambda *s: torch.randn(*s, dtype=torch.bfloat16) * 0.8)(D, 2 * Fh)), 'w_sp': _w(0.01 * (lambda *s: torch.randn(*s, dtype=torch.bfloat16) * 0.8)(S, S)), 'b_sp': _w(1.0 + 0.01 * (lambda *s: torch.randn(*s, dtype=torch.bfloat16) * 0.2)(S)), 'w_out': _w((lambda *s: torch.randn(*s, dtype=torch.bfloat16) * 0.2)(Fh, D))}
    _B_in = _B_setup()
    return {'A_x': _A_in['x'], 'A_wqkv': _A_in['wqkv'], 'A_wo': _A_in['wo'], 'B_x': _B_in['x'], 'B_w_in': _B_in['w_in'], 'B_w_sp': _B_in['w_sp'], 'B_b_sp': _B_in['b_sp'], 'B_w_out': _B_in['w_out']}

def reference(A_x, A_wqkv, A_wo, B_x, B_w_in, B_w_sp, B_b_sp, B_w_out):

    def _A_ref(x, wqkv, wo):
        B, S, D = x.shape
        H = 32
        Dh = D // H
        qkv = (lambda x, *d: x.permute(*d))((x @ wqkv).reshape(B, S, 3, H, Dh), 2, 0, 3, 1, 4)
        q, k, v = (qkv[0], qkv[1], qkv[2])
        scores = torch.einsum('bhqd,bhkd->bhqk', q, k) * Dh ** (-0.5)
        p = _softmax(scores, axis=-1)
        o = torch.einsum('bhqk,bhkd->bhqd', p, v)
        return (lambda x, *d: x.permute(*d))(o, 0, 2, 1, 3).reshape(B, S, D) @ wo

    def _B_ref(x, w_in, w_sp, b_sp, w_out):
        B, S, D = x.shape
        F = w_in.shape[-1]
        Fh = F // 2
        z = _gelu(torch.einsum('bsd,df->bsf', x, w_in))
        z1 = z[..., :Fh].clone()
        z2 = _layernorm(z[..., Fh:], axis=-1)
        g = torch.einsum('ts,bsf->btf', w_sp, z2) + b_sp[None, :, None]
        return torch.einsum('bsf,fd->bsd', z1 * g, w_out)
    x = A_x
    xn = _layernorm(x, axis=-1)
    return x + _A_ref(xn, A_wqkv, A_wo) + _B_ref(xn, B_w_in, B_w_sp, B_b_sp, B_w_out)
```

```cpp
#include <hip/hip_runtime.h>
#include <hip/hip_bf16.h>
typedef unsigned short u16;
__device__ __forceinline__ float b2f(u16 v) { return __uint_as_float(((unsigned)v) << 16); }
__device__ __forceinline__ u16 f2b(float f) { __bf16 b = (__bf16)f; return __builtin_bit_cast(u16, b); }
constexpr int S = 2048, D = 512, F = 1792, FH = 896, H = 32;

__global__ void ln_k(const u16* x, u16* y, int ld_in, int off, int n, int ld_out) {
  int r = blockIdx.x; __shared__ float red[2][4];
  float s = 0; for (int i = threadIdx.x; i < n; i += 256) s += b2f(x[r * ld_in + off + i]);
  for (int o = 32; o; o >>= 1) s += __shfl_xor(s, o);
  if ((threadIdx.x & 63) == 0) red[0][threadIdx.x >> 6] = s; __syncthreads();
  float m = (red[0][0] + red[0][1] + red[0][2] + red[0][3]) / n;
  float v = 0; for (int i = threadIdx.x; i < n; i += 256) { float d = b2f(x[r * ld_in + off + i]) - m; v += d * d; }
  for (int o = 32; o; o >>= 1) v += __shfl_xor(v, o);
  if ((threadIdx.x & 63) == 0) red[1][threadIdx.x >> 6] = v; __syncthreads();
  float rs = rsqrtf((red[1][0] + red[1][1] + red[1][2] + red[1][3]) / n + 1e-5f);
  for (int i = threadIdx.x; i < n; i += 256) y[r * ld_out + i] = f2b((b2f(x[r * ld_in + off + i]) - m) * rs);
}
__global__ void gemm_k(const u16* A, int lda, const u16* B, int ldb, int K, int mode, u16* C, int ldc, const u16* e1, int lde, const u16* bias, float* Cf) {
  __shared__ float As[16][17], Bs[16][17];
  int tx = threadIdx.x & 15, ty = threadIdx.x >> 4; int row = blockIdx.y * 16 + ty, col = blockIdx.x * 16 + tx;
  float acc = 0;
  for (int k = 0; k < K; k += 16) {
    As[ty][tx] = b2f(A[row * lda + k + tx]); Bs[ty][tx] = b2f(B[(k + ty) * ldb + col]); __syncthreads();
    for (int j = 0; j < 16; ++j) acc += As[ty][j] * Bs[j][tx];
    __syncthreads();
  }
  if (mode == 0) C[row * ldc + col] = f2b(acc);
  else if (mode == 1) { float u = 0.7978845608f * (acc + 0.044715f * acc * acc * acc); C[row * ldc + col] = f2b(0.5f * acc * (1.f + tanhf(u))); }
  else if (mode == 2) C[row * ldc + col] = f2b(b2f(e1[row * lde + col]) * (acc + b2f(bias[row])));
  else if (mode == 3) Cf[row * ldc + col] = acc;
  else C[row * ldc + col] = f2b(b2f(e1[row * lde + col]) + Cf[row * ldc + col] + acc);
}
__global__ void attn_k(const u16* qkv, u16* o) {
  int h = blockIdx.y, q = blockIdx.x * 64 + threadIdx.x;
  float qq[16], acc[16]; for (int d = 0; d < 16; ++d) { qq[d] = b2f(qkv[q * 1536 + h * 16 + d]) * 0.25f; acc[d] = 0; }
  float m = -1e30f, l = 0;
  for (int k = 0; k < S; ++k) {
    float s = 0; for (int d = 0; d < 16; ++d) s += qq[d] * b2f(qkv[k * 1536 + 512 + h * 16 + d]);
    float mn = fmaxf(m, s), a = __expf(m - mn), p = __expf(s - mn); l = l * a + p;
    for (int d = 0; d < 16; ++d) acc[d] = acc[d] * a + p * b2f(qkv[k * 1536 + 1024 + h * 16 + d]);
    m = mn;
  }
  for (int d = 0; d < 16; ++d) o[q * 512 + h * 16 + d] = f2b(acc[d] / l);
}
extern "C" void kernel_launch(void* const* d_in, const int* in_sizes, int n_in, void* d_out, int out_size, void* d_ws, size_t ws_size, hipStream_t stream) {
  const u16 *x = (const u16*)d_in[0], *wqkv = (const u16*)d_in[1], *wo = (const u16*)d_in[2], *w_in = (const u16*)d_in[4], *w_sp = (const u16*)d_in[5], *b_sp = (const u16*)d_in[6], *w_out = (const u16*)d_in[7];
  char* ws = (char*)d_ws;
  u16* xn = (u16*)ws; u16* qkv = (u16*)(ws + (4 << 20)); u16* z = (u16*)(ws + (12 << 20)); u16* z2n = (u16*)(ws + (20 << 20)); u16* u = (u16*)(ws + (24 << 20)); u16* o = (u16*)(ws + (28 << 20)); float* tmp = (float*)(ws + (32 << 20));
  ln_k<<<S, 256, 0, stream>>>(x, xn, D, 0, D, D);
  gemm_k<<<dim3(1536 / 16, S / 16), 256, 0, stream>>>(xn, D, wqkv, 1536, D, 0, qkv, 1536, nullptr, 0, nullptr, nullptr);
  gemm_k<<<dim3(F / 16, S / 16), 256, 0, stream>>>(xn, D, w_in, F, D, 1, z, F, nullptr, 0, nullptr, nullptr);
  ln_k<<<S, 256, 0, stream>>>(z, z2n, F, FH, FH, FH);
  gemm_k<<<dim3(FH / 16, S / 16), 256, 0, stream>>>(w_sp, S, z2n, FH, S, 2, u, FH, z, F, b_sp, nullptr);
  attn_k<<<dim3(S / 64, H), 64, 0, stream>>>(qkv, o);
  gemm_k<<<dim3(D / 16, S / 16), 256, 0, stream>>>(o, D, wo, D, D, 3, nullptr, D, nullptr, 0, nullptr, tmp);
  gemm_k<<<dim3(D / 16, S / 16), 256, 0, stream>>>(u, FH, w_out, D, FH, 4, (u16*)d_out, D, x, D, nullptr, tmp);
}
```

```cpp
#include <hip/hip_runtime.h>

__global__ void __launch_bounds__(1024, 4) k_fused(const void* x, const void* wqkv, const void* win, void* ws, const void* wo, const void* wout, const void* wsp, const void* bsp, void* out) {
  unsigned tid = threadIdx.x;
  asm volatile(
    "s_load_dwordx16 s[4:19], %[ka], 0\n"
    "s_load_dwordx2 s[20:21], %[ka], 64\n"
    "s_getpc_b64 s[36:37]\n"
    ".Lpc:\n"
    "v_readfirstlane_b32 s24, v0\n"
    "s_mov_b32 s33, %[bid]\n"
    "v_and_b32 v0, 63, v0\n"
    "s_lshr_b32 s26, s33, 3\n"
    "s_lshr_b32 s24, s24, 6\n"
    "s_xor_b32 s24, s24, 8\n"
    "s_cmp_eq_u32 s24, 15\n"
    "s_cbranch_scc0 .Lnopf\n"
    "s_and_b32 s38, s26, 7\n"
    "s_lshl_b32 s38, s38, 13\n"
    "v_lshl_add_u32 v2, v0, 7, s38\n"
    "v_min_u32 v2, .Lend-.Lpc-4, v2\n"
    "global_load_dword v3, v2, s[36:37]\n"
    ".Lnopf:\n"
    "s_mov_b32 s63, 0\n"
    "s_mul_i32 s27, s26, 37\n"
    "s_and_b32 s25, s33, 7\n"
    "s_lshr_b32 s27, s27, 8\n"
    "s_and_b32 s29, s33, 1\n"
    "s_mul_i32 s38, s27, 7\n"
    "s_sub_u32 s28, s26, s38\n"
    "s_lshr_b32 s38, s25, 1\n"
    "s_lshl2_add_u32 s30, s38, s27\n"
    "s_mul_i32 s38, s29, 7\n"
    "s_add_u32 s31, s38, s28\n"
    "s_mul_i32 s38, s29, 12\n"
    "s_lshl1_add_u32 s32, s28, s38\n"
    "s_cmp_lt_u32 s28, 6\n"
    "s_cselect_b32 s39, 6, 4\n"
    "s_cmp_lt_u32 s26, 28\n"
    "s_cselect_b32 s39, s39, 0\n"
    "s_sub_u32 s40, s24, 8\n"
    "s_cmp_lt_u32 s40, s39\n"
    "s_cbranch_scc0 .Lnotldr\n"
    "v_lshrrev_b32 v1, 3, v0\n"
    "v_and_b32 v2, 7, v0\n"
    "v_lshrrev_b32 v3, 4, v0\n"
    "v_and_b32 v4, 2, v1\n"
    "s_lshl1_add_u32 s41, s30, s40\n"
    "s_lshl_b32 s41, s41, 16\n"
    "s_mul_i32 s42, s40, 14\n"
    "s_add_u32 s42, s42, s31\n"
    "s_sub_u32 s42, s42, 28\n"
    "s_add_u32 s43, s32, s40\n"
    "s_sub_u32 s43, s43, 4\n"
    "s_cmp_lt_u32 s40, 4\n"
    "s_cselect_b32 s42, s42, s43\n"
    "s_movk_i32 s52, 0xc00\n"
    "s_cselect_b32 s52, 0xe00, s52\n"
    "s_lshl_b32 s42, s42, 7\n"
    "s_lshl_b32 s53, s52, 6\n"
    "s_cmp_lt_u32 s40, 2\n"
    "s_cselect_b32 s43, s41, s42\n"
    "s_cselect_b32 s52, 0x400, s52\n"
    "s_cselect_b64 vcc, -1, 0\n"
    "s_cselect_b32 s53, 128, s53\n"
    "s_cselect_b32 s41, s28, s27\n"
    "v_cndmask_b32 v3, v4, v3, vcc\n"
    "v_xor_b32 v2, v2, v3\n"
    "v_lshlrev_b32 v2, 4, v2\n"
    "v_mad_u32_u24 v1, v1, s52, v2\n"
    "v_xor_b32 v2, 64, v1\n"
    "s_lshl_b32 s54, s52, 3\n"
    "s_lshl_b32 s55, s40, 13\n"
    "v_add_u32 v2, s54, v2\n"
    "s_lshl_b32 s54, s52, 4\n"
    "s_add_u32 s42, s41, 0\n"
    "s_and_b32 s42, s42, 3\n"
    "s_mul_i32 s58, s42, s54\n"
    "s_lshl_b32 s38, s42, 11\n"
    "s_add_u32 s58, s58, s43\n"
    "s_add_u32 s42, s41, 1\n"
    "s_and_b32 s42, s42, 3\n"
    "s_mul_i32 s59, s42, s54\n"
    "s_lshl_b32 s39, s42, 11\n"
    "s_add_u32 s59, s59, s43\n"
    "s_add_u32 s42, s41, 2\n"
    "s_and_b32 s42, s42, 3\n"
    "s_mul_i32 s60, s42, s54\n"
    "s_lshl_b32 s50, s42, 11\n"
    "s_add_u32 s60, s60, s43\n"
    "s_add_u32 s42, s41, 3\n"
    "s_and_b32 s42, s42, 3\n"
    "s_mul_i32 s61, s42, s54\n"
    "s_lshl_b32 s51, s42, 11\n"
    "s_add_u32 s61, s61, s43\n"
    "s_mov_b32 s37, s55\n"
    "s_mov_b32 s36, 0\n"
    "s_cmp_lt_u32 s40, 4\n"
    "s_waitcnt lgkmcnt(0)\n"
    "s_cselect_b64 s[48:49], s[8:9], s[6:7]\n"
    "s_cmp_lt_u32 s40, 2\n"
    "s_cselect_b64 s[48:49], s[4:5], s[48:49]\n"
    ".Lld_1:\n"
    "s_add_u32 m0, s37, s38\n"
    "s_add_u32 s44, s48, s58\n"
    "s_addc_u32 s45, s49, 0\n"
    "global_load_lds_dwordx4 v1, s[44:45]\n"
    "s_add_u32 m0, m0, 0x400\n"
    "s_add_u32 s36, s36, 1\n"
    "global_load_lds_dwordx4 v2, s[44:45]\n"
    "s_add_u32 m0, s37, s39\n"
    "s_add_u32 s44, s48, s59\n"
    "s_addc_u32 s45, s49, 0\n"
    "global_load_lds_dwordx4 v1, s[44:45]\n"
    "s_add_u32 m0, m0, 0x400\n"
    "s_nop 0\n"
    "global_load_lds_dwordx4 v2, s[44:45]\n"
    "s_add_u32 m0, s37, s50\n"
    "s_add_u32 s44, s48, s60\n"
    "s_addc_u32 s45, s49, 0\n"
    "global_load_lds_dwordx4 v1, s[44:45]\n"
    "s_add_u32 m0, m0, 0x400\n"
    "s_nop 0\n"
    "global_load_lds_dwordx4 v2, s[44:45]\n"
    "s_add_u32 m0, s37, s51\n"
    "s_add_u32 s44, s48, s61\n"
    "s_addc_u32 s45, s49, 0\n"
    "global_load_lds_dwordx4 v1, s[44:45]\n"
    "s_add_u32 m0, m0, 0x400\n"
    "s_nop 0\n"
    "global_load_lds_dwordx4 v2, s[44:45]\n"
    "s_add_u32 s48, s48, s53\n"
    "s_addc_u32 s49, s49, 0\n"
    "s_add_u32 s37, s37, 49152\n"
    "s_bitcmp1_b32 0x48, s36\n"
    "s_cselect_b32 s37, s55, s37\n"
    "s_cmp_eq_u32 s36, 1\n"
    "s_cbranch_scc0 .Lld_2\n"
    "s_waitcnt vmcnt(6)\n"
    "s_branch .Lld_1\n"
    ".Lld_2:\n"
    "s_cmp_lt_u32 s36, 8\n"
    "s_waitcnt vmcnt(8)\n"
    "s_barrier\n"
    "s_cbranch_scc1 .Lld_1\n"
    "s_waitcnt vmcnt(0)\n"
    "s_barrier\n"
    "s_barrier\n"
    "s_branch .Lph2\n"
    ".Lnotldr:\n"
    "v_mov_b32 v13, 157696\n"
    "s_cmp_lt_u32 s26, 28\n"
    "s_cbranch_scc0 .Lspare\n"
    "s_lshl_b32 s39, s39, 1\n"
    "s_sub_u32 s39, s39, 4\n"
    "s_cmp_lt_u32 s24, s39\n"
    "s_cbranch_scc0 .Lidle\n"
    "s_lshr_b32 s53, s24, 1\n"
    "s_and_b32 s54, s24, 1\n"
    "s_and_b32 s55, s53, 1\n"
    "s_add_u32 s55, s55, s32\n"
    "s_lshl1_add_u32 s56, s30, s54\n"
    "s_lshl_b32 s56, s56, 1\n"
    "s_cmp_lt_u32 s55, 16\n"
    "s_cselect_b32 s39, 2, 3\n"
    "s_cmp_lt_u32 s53, 2\n"
    "s_cselect_b32 s39, s53, s39\n"
    "v_and_b32 v14, 15, v0\n"
    "v_lshrrev_b32 v15, 4, v0\n"
    "v_and_b32 v12, 0x30, v0\n"
    "v_lshlrev_b32 v12, 6, v12\n"
    "v_and_b32 v1, 12, v0\n"
    "v_lshl_or_b32 v12, v1, 5, v12\n"
    "v_and_b32 v1, 24, v0\n"
    "v_lshl_or_b32 v12, v1, 2, v12\n"
    "v_and_b32 v1, 3, v0\n"
    "v_lshl_or_b32 v12, v1, 3, v12\n"
    "v_lshrrev_b32 v1, 1, v14\n"
    "v_xor_b32 v1, v1, v15\n"
    "v_lshlrev_b32 v1, 4, v1\n"
    "v_lshl_or_b32 v1, v14, 7, v1\n"
    "s_lshl_b32 s40, s54, 13\n"
    "v_add_u32 v2, s40, v1\n"
    "v_xor_b32 v3, 64, v2\n"
    "s_lshl_b32 s41, s53, 13\n"
    "s_add_u32 s41, s41, 16384\n"
    "v_add_u32 v4, s41, v12\n"
    "v_xor_b32 v5, 32, v4\n"
    "v_xor_b32 v6, 64, v4\n"
    "v_xor_b32 v7, 0x60, v4\n"
    "s_lshl_b32 s41, s54, 9\n"
    "s_add_u32 s41, s41, 155648\n"
    "v_lshl_add_u32 v8, v14, 3, s41\n"
    "s_lshl_b32 s41, s53, 8\n"
    "s_add_u32 s41, s41, 156672\n"
    "v_lshl_add_u32 v9, v15, 4, s41\n"
    "s_and_b32 s41, s24, 3\n"
    "s_lshl_b32 s41, s41, 13\n"
    "s_add_u32 s41, s41, 98304\n"
    "s_add_u32 s42, s40, 65536\n"
    "s_cmp_eq_u32 s39, 1\n"
    "s_cselect_b32 s41, s42, s41\n"
    "v_lshlrev_b32 v11, 7, v14\n"
    "v_lshl_or_b32 v11, v15, 3, v11\n"
    "v_and_b32 v1, 2, v14\n"
    "v_lshl_or_b32 v11, v1, 4, v11\n"
    "v_and_b32 v1, 8, v14\n"
    "v_lshl_or_b32 v11, v1, 3, v11\n"
    "v_add_u32 v11, s41, v11\n"
    "v_add_u32 v12, s41, v12\n"
    "s_and_b32 s60, s55, 7\n"
    "s_lshl_b32 s60, s60, 8\n"
    "s_add_u32 s60, s60, s56\n"
    "s_lshl_b32 s60, s60, 11\n"
    "s_add_u32 s42, s60, 2097152\n"
    "s_movk_i32 s41, 0x800\n"
    "s_mov_b32 s52, 0x40000\n"
    "s_mov_b32 s57, 0x20000\n"
    "s_movk_i32 s58, 0x200\n"
    "s_mov_b32 s59, 0\n"
    "s_movk_i32 s61, 0x100\n"
    "s_lshl3_add_u32 s60, s56, s55\n"
    "s_lshl_b32 s60, s60, 12\n"
    "s_cmp_lt_u32 s55, 8\n"
    "s_cselect_b32 s42, s60, s42\n"
    "s_cselect_b32 s62, 0x3eb8aa3b, 1.0\n"
    "s_cselect_b32 s41, 0x8000, s41\n"
    "s_cselect_b32 s52, 0x800, s52\n"
    "s_cselect_b32 s57, 0x200, s57\n"
    "s_cselect_b32 s58, 0x100, s58\n"
    "s_cselect_b32 s61, 0x400, s61\n"
    "s_cmp_eq_u32 s39, 3\n"
    "s_cselect_b32 s41, 0x20000, s41\n"
    "s_cselect_b32 s57, 0x200, s57\n"
    "s_cselect_b32 s58, 0x100, s58\n"
    "s_cselect_b32 s59, 0x400, s59\n"
    "s_cselect_b64 vcc, -1, 0\n"
    "s_lshl_b32 s60, s31, 6\n"
    "s_add_u32 s60, s60, s56\n"
    "s_lshl_b32 s60, s60, 9\n"
    "s_add_u32 s60, s60, 23134208\n"
    "s_cmp_eq_u32 s39, 1\n"
    "s_cselect_b32 s42, s60, s42\n"
    "s_cselect_b32 s57, 0x100, s57\n"
    "s_cselect_b32 s58, 0x200, s58\n"
    "s_add_u32 s60, s40, 81920\n"
    "s_cmp_eq_u32 s39, 0\n"
    "s_cselect_b32 s57, 0x200, s57\n"
    "s_cselect_b32 s58, 0x100, s58\n"
    "s_cselect_b32 s59, s60, s59\n"
    "v_and_b32 v10, 3, v0\n"
    "v_and_b32 v1, 4, v0\n"
    "v_lshl_or_b32 v10, v1, 1, v10\n"
    "v_bfe_u32 v1, v0, 3, 1\n"
    "v_lshl_or_b32 v10, v1, 2, v10\n"
    "v_cndmask_b32 v10, v14, v10, vcc\n"
    "v_lshl_add_u32 v10, v10, 4, s59\n"
    "v_and_b32 v1, 1, v15\n"
    "v_mad_u32_u24 v10, v1, s57, v10\n"
    "v_lshrrev_b32 v1, 1, v15\n"
    "v_mad_u32_u24 v10, v1, s58, v10\n"
    "v_add_u32 v1, s61, v10\n"
    "s_mov_b32 s40, s62\n"
    "s_mov_b32 s38, 49152\n"
    "s_waitcnt lgkmcnt(0)\n"
    "s_add_u32 s42, s10, s42\n"
    "s_addc_u32 s43, s11, 0\n"
    "v_mov_b64 v[64:65], 0\n"
    "v_mov_b64 v[66:67], 0\n"
    "v_mov_b64 v[68:69], 0\n"
    "v_mov_b64 v[70:71], 0\n"
    "v_mov_b64 v[72:73], 0\n"
    "v_mov_b64 v[74:75], 0\n"
    "v_mov_b64 v[76:77], 0\n"
    "v_mov_b64 v[78:79], 0\n"
    "v_mov_b64 v[80:81], 0\n"
    "v_mov_b64 v[82:83], 0\n"
    "v_mov_b64 v[84:85], 0\n"
    "v_mov_b64 v[86:87], 0\n"
    "v_mov_b64 v[88:89], 0\n"
    "v_mov_b64 v[90:91], 0\n"
    "v_mov_b64 v[92:93], 0\n"
    "v_mov_b64 v[94:95], 0\n"
    "v_mov_b64 v[96:97], 0\n"
    "v_mov_b64 v[98:99], 0\n"
    "v_mov_b64 v[100:101], 0\n"
    "v_mov_b64 v[102:103], 0\n"
    "v_mov_b64 v[104:105], 0\n"
    "v_mov_b64 v[106:107], 0\n"
    "v_mov_b64 v[108:109], 0\n"
    "v_mov_b64 v[110:111], 0\n"
    "v_mov_b64 v[112:113], 0\n"
    "v_mov_b64 v[114:115], 0\n"
    "v_mov_b64 v[116:117], 0\n"
    "v_mov_b64 v[118:119], 0\n"
    "v_mov_b64 v[120:121], 0\n"
    "v_mov_b64 v[122:123], 0\n"
    "v_mov_b64 v[124:125], 0\n"
    "v_mov_b64 v[126:127], 0\n"
    "s_mov_b32 s36, 0\n"
    "s_barrier\n"
    ".Lmm_1:\n"
    "ds_read_b128 v[32:35], v2 offset:0\n"
    "ds_read_b128 v[36:39], v2 offset:2048\n"
    "ds_read_b128 v[40:43], v2 offset:4096\n"
    "ds_read_b128 v[44:47], v2 offset:6144\n"
    "ds_read_b64_tr_b16 v[48:49], v4 offset:0\n"
    "ds_read_b64_tr_b16 v[50:51], v4 offset:512\n"
    "ds_read_b64_tr_b16 v[52:53], v5 offset:0\n"
    "ds_read_b64_tr_b16 v[54:55], v5 offset:512\n"
    "ds_read_b64_tr_b16 v[56:57], v6 offset:0\n"
    "ds_read_b64_tr_b16 v[58:59], v6 offset:512\n"
    "ds_read_b64_tr_b16 v[60:61], v7 offset:0\n"
    "ds_read_b64_tr_b16 v[62:63], v7 offset:512\n"
    "s_waitcnt lgkmcnt(6)\n"
    "v_mfma_f32_16x16x32_bf16 v[64:67], v[48:51], v[32:35], v[64:67]\n"
    "s_setprio 1\n"
    "v_mfma_f32_16x16x32_bf16 v[80:83], v[48:51], v[36:39], v[80:83]\n"
    "v_mfma_f32_16x16x32_bf16 v[96:99], v[48:51], v[40:43], v[96:99]\n"
    "v_mfma_f32_16x16x32_bf16 v[112:115], v[48:51], v[44:47], v[112:115]\n"
    "s_waitcnt lgkmcnt(4)\n"
    "v_mfma_f32_16x16x32_bf16 v[68:71], v[52:55], v[32:35], v[68:71]\n"
    "v_mfma_f32_16x16x32_bf16 v[84:87], v[52:55], v[36:39], v[84:87]\n"
    "v_mfma_f32_16x16x32_bf16 v[100:103], v[52:55], v[40:43], v[100:103]\n"
    "v_mfma_f32_16x16x32_bf16 v[116:119], v[52:55], v[44:47], v[116:119]\n"
    "s_waitcnt lgkmcnt(2)\n"
    "v_mfma_f32_16x16x32_bf16 v[72:75], v[56:59], v[32:35], v[72:75]\n"
    "v_mfma_f32_16x16x32_bf16 v[88:91], v[56:59], v[36:39], v[88:91]\n"
    "v_mfma_f32_16x16x32_bf16 v[104:107], v[56:59], v[40:43], v[104:107]\n"
    "v_mfma_f32_16x16x32_bf16 v[120:123], v[56:59], v[44:47], v[120:123]\n"
    "s_waitcnt lgkmcnt(0)\n"
    "v_mfma_f32_16x16x32_bf16 v[76:79], v[60:63], v[32:35], v[76:79]\n"
    "v_mfma_f32_16x16x32_bf16 v[92:95], v[60:63], v[36:39], v[92:95]\n"
    "v_mfma_f32_16x16x32_bf16 v[108:111], v[60:63], v[40:43], v[108:111]\n"
    "v_mfma_f32_16x16x32_bf16 v[124:127], v[60:63], v[44:47], v[124:127]\n"
    "s_setprio 0\n"
    "ds_read_b128 v[32:35], v3 offset:0\n"
    "ds_read_b128 v[36:39], v3 offset:2048\n"
    "ds_read_b128 v[40:43], v3 offset:4096\n"
    "ds_read_b128 v[44:47], v3 offset:6144\n"
    "ds_read_b64_tr_b16 v[48:49], v4 offset:4096\n"
    "ds_read_b64_tr_b16 v[50:51], v4 offset:4608\n"
    "ds_read_b64_tr_b16 v[52:53], v5 offset:4096\n"
    "ds_read_b64_tr_b16 v[54:55], v5 offset:4608\n"
    "ds_read_b64_tr_b16 v[56:57], v6 offset:4096\n"
    "ds_read_b64_tr_b16 v[58:59], v6 offset:4608\n"
    "ds_read_b64_tr_b16 v[60:61], v7 offset:4096\n"
    "ds_read_b64_tr_b16 v[62:63], v7 offset:4608\n"
    "s_add_u32 s36, s36, 1\n"
    "s_bitcmp1_b32 0x48, s36\n"
    "s_cselect_b32 s37, -98304, s38\n"
    "s_cmp_ge_u32 s39, 2\n"
    "s_cbranch_scc1 .Lmm_2\n"
    "s_waitcnt lgkmcnt(0)\n"
    "s_barrier\n"
    ".Lmm_2:\n"
    "v_add_u32 v2, s37, v2\n"
    "v_add_u32 v3, s37, v3\n"
    "v_add_u32 v4, s37, v4\n"
    "v_add_u32 v5, s37, v5\n"
    "v_add_u32 v6, s37, v6\n"
    "v_add_u32 v7, s37, v7\n"
    "s_waitcnt lgkmcnt(6)\n"
    "v_mfma_f32_16x16x32_bf16 v[64:67], v[48:51], v[32:35], v[64:67]\n"
    "s_setprio 1\n"
    "v_mfma_f32_16x16x32_bf16 v[80:83], v[48:51], v[36:39], v[80:83]\n"
    "v_mfma_f32_16x16x32_bf16 v[96:99], v[48:51], v[40:43], v[96:99]\n"
    "v_mfma_f32_16x16x32_bf16 v[112:115], v[48:51], v[44:47], v[112:115]\n"
    "s_waitcnt lgkmcnt(4)\n"
    "v_mfma_f32_16x16x32_bf16 v[68:71], v[52:55], v[32:35], v[68:71]\n"
    "v_mfma_f32_16x16x32_bf16 v[84:87], v[52:55], v[36:39], v[84:87]\n"
    "v_mfma_f32_16x16x32_bf16 v[100:103], v[52:55], v[40:43], v[100:103]\n"
    "v_mfma_f32_16x16x32_bf16 v[116:119], v[52:55], v[44:47], v[116:119]\n"
    "s_waitcnt lgkmcnt(2)\n"
    "v_mfma_f32_16x16x32_bf16 v[72:75], v[56:59], v[32:35], v[72:75]\n"
    "v_mfma_f32_16x16x32_bf16 v[88:91], v[56:59], v[36:39], v[88:91]\n"
    "v_mfma_f32_16x16x32_bf16 v[104:107], v[56:59], v[40:43], v[104:107]\n"
    "v_mfma_f32_16x16x32_bf16 v[120:123], v[56:59], v[44:47], v[120:123]\n"
    "s_waitcnt lgkmcnt(0)\n"
    "v_mfma_f32_16x16x32_bf16 v[76:79], v[60:63], v[32:35], v[76:79]\n"
    "v_mfma_f32_16x16x32_bf16 v[92:95], v[60:63], v[36:39], v[92:95]\n"
    "v_mfma_f32_16x16x32_bf16 v[108:111], v[60:63], v[40:43], v[108:111]\n"
    "v_mfma_f32_16x16x32_bf16 v[124:127], v[60:63], v[44:47], v[124:127]\n"
    "s_setprio 0\n"
    "s_cbranch_scc0 .Lmm_3\n"
    "s_barrier\n"
    ".Lmm_3:\n"
    "s_cmp_lt_u32 s36, 8\n"
    "s_cbranch_scc1 .Lmm_1\n"
    "s_waitcnt vmcnt(0)\n"
    "s_setprio 3\n"
    "s_cmp_lt_u32 s39, 2\n"
    "s_cbranch_scc0 .Lep_p\n"
    "s_setprio 2\n"
    ".Lep_p:\n"
    "ds_read_b64 v[14:15], v13 offset:72\n"
    "ds_read_b64 v[16:17], v8 offset:0\n"
    "ds_read_b64 v[18:19], v8 offset:128\n"
    "ds_read_b64 v[20:21], v8 offset:256\n"
    "ds_read_b64 v[22:23], v8 offset:384\n"
    "ds_read_b128 v[32:35], v9 offset:0\n"
    "ds_read_b128 v[36:39], v9 offset:64\n"
    "ds_read_b128 v[40:43], v9 offset:128\n"
    "ds_read_b128 v[44:47], v9 offset:192\n"
    "s_mov_b64 s[44:45], s[42:43]\n"
    "s_add_u32 s46, s44, s41\n"
    "s_addc_u32 s47, s45, 0\n"
    "s_add_u32 s48, s44, s52\n"
    "s_addc_u32 s49, s45, 0\n"
    "s_add_u32 s50, s46, s52\n"
    "s_addc_u32 s51, s47, 0\n"
    "v_xor_b32 v24, 0, v11\n"
    "v_xor_b32 v25, 32, v11\n"
    "v_xor_b32 v26, 64, v11\n"
    "v_xor_b32 v27, 96, v11\n"
    "v_xor_b32 v28, 0, v12\n"
    "v_xor_b32 v29, 32, v12\n"
    "v_xor_b32 v30, 64, v12\n"
    "v_xor_b32 v31, 96, v12\n"
    "s_waitcnt lgkmcnt(0)\n"
    "s_cmp_eq_u32 s39, 2\n"
    "s_cbranch_scc0 .Lep_6\n"
    "v_mul_f32 v17, s40, v17\n"
    "v_mul_f32 v19, s40, v19\n"
    "v_mul_f32 v21, s40, v21\n"
    "v_mul_f32 v23, s40, v23\n"
    "v_pk_fma_f32 v[64:65], v[32:33], v[16:17], v[64:65] op_sel_hi:[1,0,1]\n"
    "v_pk_fma_f32 v[66:67], v[34:35], v[16:17], v[66:67] op_sel_hi:[1,0,1]\n"
    "v_pk_fma_f32 v[68:69], v[36:37], v[16:17], v[68:69] op_sel_hi:[1,0,1]\n"
    "v_pk_fma_f32 v[70:71], v[38:39], v[16:17], v[70:71] op_sel_hi:[1,0,1]\n"
    "v_pk_fma_f32 v[72:73], v[40:41], v[16:17], v[72:73] op_sel_hi:[1,0,1]\n"
    "v_pk_fma_f32 v[74:75], v[42:43], v[16:17], v[74:75] op_sel_hi:[1,0,1]\n"
    "v_pk_fma_f32 v[76:77], v[44:45], v[16:17], v[76:77] op_sel_hi:[1,0,1]\n"
    "v_pk_fma_f32 v[78:79], v[46:47], v[16:17], v[78:79] op_sel_hi:[1,0,1]\n"
    "v_pk_mul_f32 v[64:65], v[64:65], v[16:17] op_sel:[0,1] op_sel_hi:[1,1]\n"
    "v_pk_mul_f32 v[66:67], v[66:67], v[16:17] op_sel:[0,1] op_sel_hi:[1,1]\n"
    "v_pk_mul_f32 v[68:69], v[68:69], v[16:17] op_sel:[0,1] op_sel_hi:[1,1]\n"
    "v_pk_mul_f32 v[70:71], v[70:71], v[16:17] op_sel:[0,1] op_sel_hi:[1,1]\n"
    "v_pk_mul_f32 v[72:73], v[72:73], v[16:17] op_sel:[0,1] op_sel_hi:[1,1]\n"
    "v_pk_mul_f32 v[74:75], v[74:75], v[16:17] op_sel:[0,1] op_sel_hi:[1,1]\n"
    "v_pk_mul_f32 v[76:77], v[76:77], v[16:17] op_sel:[0,1] op_sel_hi:[1,1]\n"
    "v_pk_mul_f32 v[78:79], v[78:79], v[16:17] op_sel:[0,1] op_sel_hi:[1,1]\n"
    "v_cvt_pk_bf16_f32 v48, v64, v65\n"
    "v_cvt_pk_bf16_f32 v49, v66, v67\n"
    "v_cvt_pk_bf16_f32 v50, v68, v69\n"
    "v_cvt_pk_bf16_f32 v51, v70, v71\n"
    "v_cvt_pk_bf16_f32 v52, v72, v73\n"
    "v_cvt_pk_bf16_f32 v53, v74, v75\n"
    "v_cvt_pk_bf16_f32 v54, v76, v77\n"
    "v_cvt_pk_bf16_f32 v55, v78, v79\n"
    "v_permlane16_swap_b32 v48, v50\n"
    "v_permlane16_swap_b32 v49, v51\n"
    "v_permlane16_swap_b32 v52, v54\n"
    "v_permlane16_swap_b32 v53, v55\n"
    "global_store_dwordx4 v10, v[48:51], s[44:45] sc1\n"
    "global_store_dwordx4 v10, v[52:55], s[48:49] sc1\n"
    "v_pk_fma_f32 v[80:81], v[32:33], v[18:19], v[80:81] op_sel_hi:[1,0,1]\n"
    "v_pk_fma_f32 v[82:83], v[34:35], v[18:19], v[82:83] op_sel_hi:[1,0,1]\n"
    "v_pk_fma_f32 v[84:85], v[36:37], v[18:19], v[84:85] op_sel_hi:[1,0,1]\n"
    "v_pk_fma_f32 v[86:87], v[38:39], v[18:19], v[86:87] op_sel_hi:[1,0,1]\n"
    "v_pk_fma_f32 v[88:89], v[40:41], v[18:19], v[88:89] op_sel_hi:[1,0,1]\n"
    "v_pk_fma_f32 v[90:91], v[42:43], v[18:19], v[90:91] op_sel_hi:[1,0,1]\n"
    "v_pk_fma_f32 v[92:93], v[44:45], v[18:19], v[92:93] op_sel_hi:[1,0,1]\n"
    "v_pk_fma_f32 v[94:95], v[46:47], v[18:19], v[94:95] op_sel_hi:[1,0,1]\n"
    "v_pk_mul_f32 v[80:81], v[80:81], v[18:19] op_sel:[0,1] op_sel_hi:[1,1]\n"
    "v_pk_mul_f32 v[82:83], v[82:83], v[18:19] op_sel:[0,1] op_sel_hi:[1,1]\n"
    "v_pk_mul_f32 v[84:85], v[84:85], v[18:19] op_sel:[0,1] op_sel_hi:[1,1]\n"
    "v_pk_mul_f32 v[86:87], v[86:87], v[18:19] op_sel:[0,1] op_sel_hi:[1,1]\n"
    "v_pk_mul_f32 v[88:89], v[88:89], v[18:19] op_sel:[0,1] op_sel_hi:[1,1]\n"
    "v_pk_mul_f32 v[90:91], v[90:91], v[18:19] op_sel:[0,1] op_sel_hi:[1,1]\n"
    "v_pk_mul_f32 v[92:93], v[92:93], v[18:19] op_sel:[0,1] op_sel_hi:[1,1]\n"
    "v_pk_mul_f32 v[94:95], v[94:95], v[18:19] op_sel:[0,1] op_sel_hi:[1,1]\n"
    "v_cvt_pk_bf16_f32 v56, v80, v81\n"
    "v_cvt_pk_bf16_f32 v57, v82, v83\n"
    "v_cvt_pk_bf16_f32 v58, v84, v85\n"
    "v_cvt_pk_bf16_f32 v59, v86, v87\n"
    "v_cvt_pk_bf16_f32 v60, v88, v89\n"
    "v_cvt_pk_bf16_f32 v61, v90, v91\n"
    "v_cvt_pk_bf16_f32 v62, v92, v93\n"
    "v_cvt_pk_bf16_f32 v63, v94, v95\n"
    "v_permlane16_swap_b32 v56, v58\n"
    "v_permlane16_swap_b32 v57, v59\n"
    "v_permlane16_swap_b32 v60, v62\n"
    "v_permlane16_swap_b32 v61, v63\n"
    "global_store_dwordx4 v1, v[56:59], s[44:45] sc1\n"
    "global_store_dwordx4 v1, v[60:63], s[48:49] sc1\n"
    "v_pk_fma_f32 v[96:97], v[32:33], v[20:21], v[96:97] op_sel_hi:[1,0,1]\n"
    "v_pk_fma_f32 v[98:99], v[34:35], v[20:21], v[98:99] op_sel_hi:[1,0,1]\n"
    "v_pk_fma_f32 v[100:101], v[36:37], v[20:21], v[100:101] op_sel_hi:[1,0,1]\n"
    "v_pk_fma_f32 v[102:103], v[38:39], v[20:21], v[102:103] op_sel_hi:[1,0,1]\n"
    "v_pk_fma_f32 v[104:105], v[40:41], v[20:21], v[104:105] op_sel_hi:[1,0,1]\n"
    "v_pk_fma_f32 v[106:107], v[42:43], v[20:21], v[106:107] op_sel_hi:[1,0,1]\n"
    "v_pk_fma_f32 v[108:109], v[44:45], v[20:21], v[108:109] op_sel_hi:[1,0,1]\n"
    "v_pk_fma_f32 v[110:111], v[46:47], v[20:21], v[110:111] op_sel_hi:[1,0,1]\n"
    "v_pk_mul_f32 v[96:97], v[96:97], v[20:21] op_sel:[0,1] op_sel_hi:[1,1]\n"
    "v_pk_mul_f32 v[98:99], v[98:99], v[20:21] op_sel:[0,1] op_sel_hi:[1,1]\n"
    "v_pk_mul_f32 v[100:101], v[100:101], v[20:21] op_sel:[0,1] op_sel_hi:[1,1]\n"
    "v_pk_mul_f32 v[102:103], v[102:103], v[20:21] op_sel:[0,1] op_sel_hi:[1,1]\n"
    "v_pk_mul_f32 v[104:105], v[104:105], v[20:21] op_sel:[0,1] op_sel_hi:[1,1]\n"
    "v_pk_mul_f32 v[106:107], v[106:107], v[20:21] op_sel:[0,1] op_sel_hi:[1,1]\n"
    "v_pk_mul_f32 v[108:109], v[108:109], v[20:21] op_sel:[0,1] op_sel_hi:[1,1]\n"
    "v_pk_mul_f32 v[110:111], v[110:111], v[20:21] op_sel:[0,1] op_sel_hi:[1,1]\n"
    "v_cvt_pk_bf16_f32 v48, v96, v97\n"
    "v_cvt_pk_bf16_f32 v49, v98, v99\n"
    "v_cvt_pk_bf16_f32 v50, v100, v101\n"
    "v_cvt_pk_bf16_f32 v51, v102, v103\n"
    "v_cvt_pk_bf16_f32 v52, v104, v105\n"
    "v_cvt_pk_bf16_f32 v53, v106, v107\n"
    "v_cvt_pk_bf16_f32 v54, v108, v109\n"
    "v_cvt_pk_bf16_f32 v55, v110, v111\n"
    "v_permlane16_swap_b32 v48, v50\n"
    "v_permlane16_swap_b32 v49, v51\n"
    "v_permlane16_swap_b32 v52, v54\n"
    "v_permlane16_swap_b32 v53, v55\n"
    "global_store_dwordx4 v10, v[48:51], s[46:47] sc1\n"
    "global_store_dwordx4 v10, v[52:55], s[50:51] sc1\n"
    "v_pk_fma_f32 v[112:113], v[32:33], v[22:23], v[112:113] op_sel_hi:[1,0,1]\n"
    "v_pk_fma_f32 v[114:115], v[34:35], v[22:23], v[114:115] op_sel_hi:[1,0,1]\n"
    "v_pk_fma_f32 v[116:117], v[36:37], v[22:23], v[116:117] op_sel_hi:[1,0,1]\n"
    "v_pk_fma_f32 v[118:119], v[38:39], v[22:23], v[118:119] op_sel_hi:[1,0,1]\n"
    "v_pk_fma_f32 v[120:121], v[40:41], v[22:23], v[120:121] op_sel_hi:[1,0,1]\n"
    "v_pk_fma_f32 v[122:123], v[42:43], v[22:23], v[122:123] op_sel_hi:[1,0,1]\n"
    "v_pk_fma_f32 v[124:125], v[44:45], v[22:23], v[124:125] op_sel_hi:[1,0,1]\n"
    "v_pk_fma_f32 v[126:127], v[46:47], v[22:23], v[126:127] op_sel_hi:[1,0,1]\n"
    "v_pk_mul_f32 v[112:113], v[112:113], v[22:23] op_sel:[0,1] op_sel_hi:[1,1]\n"
    "v_pk_mul_f32 v[114:115], v[114:115], v[22:23] op_sel:[0,1] op_sel_hi:[1,1]\n"
    "v_pk_mul_f32 v[116:117], v[116:117], v[22:23] op_sel:[0,1] op_sel_hi:[1,1]\n"
    "v_pk_mul_f32 v[118:119], v[118:119], v[22:23] op_sel:[0,1] op_sel_hi:[1,1]\n"
    "v_pk_mul_f32 v[120:121], v[120:121], v[22:23] op_sel:[0,1] op_sel_hi:[1,1]\n"
    "v_pk_mul_f32 v[122:123], v[122:123], v[22:23] op_sel:[0,1] op_sel_hi:[1,1]\n"
    "v_pk_mul_f32 v[124:125], v[124:125], v[22:23] op_sel:[0,1] op_sel_hi:[1,1]\n"
    "v_pk_mul_f32 v[126:127], v[126:127], v[22:23] op_sel:[0,1] op_sel_hi:[1,1]\n"
    "v_cvt_pk_bf16_f32 v56, v112, v113\n"
    "v_cvt_pk_bf16_f32 v57, v114, v115\n"
    "v_cvt_pk_bf16_f32 v58, v116, v117\n"
    "v_cvt_pk_bf16_f32 v59, v118, v119\n"
    "v_cvt_pk_bf16_f32 v60, v120, v121\n"
    "v_cvt_pk_bf16_f32 v61, v122, v123\n"
    "v_cvt_pk_bf16_f32 v62, v124, v125\n"
    "v_cvt_pk_bf16_f32 v63, v126, v127\n"
    "v_permlane16_swap_b32 v56, v58\n"
    "v_permlane16_swap_b32 v57, v59\n"
    "v_permlane16_swap_b32 v60, v62\n"
    "v_permlane16_swap_b32 v61, v63\n"
    "global_store_dwordx4 v1, v[56:59], s[46:47] sc1\n"
    "global_store_dwordx4 v1, v[60:63], s[50:51] sc1\n"
    "s_branch .Lep_7\n"
    ".Lep_6:\n"
    "s_cmp_eq_u32 s39, 3\n"
    "s_cbranch_scc0 .Lep_8\n"
    "v_pk_fma_f32 v[64:65], v[32:33], v[16:17], v[64:65] op_sel_hi:[1,0,1]\n"
    "v_pk_fma_f32 v[66:67], v[34:35], v[16:17], v[66:67] op_sel_hi:[1,0,1]\n"
    "v_pk_fma_f32 v[68:69], v[36:37], v[16:17], v[68:69] op_sel_hi:[1,0,1]\n"
    "v_pk_fma_f32 v[70:71], v[38:39], v[16:17], v[70:71] op_sel_hi:[1,0,1]\n"
    "v_pk_fma_f32 v[72:73], v[40:41], v[16:17], v[72:73] op_sel_hi:[1,0,1]\n"
    "v_pk_fma_f32 v[74:75], v[42:43], v[16:17], v[74:75] op_sel_hi:[1,0,1]\n"
    "v_pk_fma_f32 v[76:77], v[44:45], v[16:17], v[76:77] op_sel_hi:[1,0,1]\n"
    "v_pk_fma_f32 v[78:79], v[46:47], v[16:17], v[78:79] op_sel_hi:[1,0,1]\n"
    "v_pk_mul_f32 v[64:65], v[64:65], v[16:17] op_sel:[0,1] op_sel_hi:[1,1]\n"
    "v_pk_mul_f32 v[66:67], v[66:67], v[16:17] op_sel:[0,1] op_sel_hi:[1,1]\n"
    "v_pk_mul_f32 v[68:69], v[68:69], v[16:17] op_sel:[0,1] op_sel_hi:[1,1]\n"
    "v_pk_mul_f32 v[70:71], v[70:71], v[16:17] op_sel:[0,1] op_sel_hi:[1,1]\n"
    "v_pk_mul_f32 v[72:73], v[72:73], v[16:17] op_sel:[0,1] op_sel_hi:[1,1]\n"
    "v_pk_mul_f32 v[74:75], v[74:75], v[16:17] op_sel:[0,1] op_sel_hi:[1,1]\n"
    "v_pk_mul_f32 v[76:77], v[76:77], v[16:17] op_sel:[0,1] op_sel_hi:[1,1]\n"
    "v_pk_mul_f32 v[78:79], v[78:79], v[16:17] op_sel:[0,1] op_sel_hi:[1,1]\n"
    "v_cvt_pk_bf16_f32 v48, v64, v65\n"
    "v_cvt_pk_bf16_f32 v49, v66, v67\n"
    "v_cvt_pk_bf16_f32 v50, v68, v69\n"
    "v_cvt_pk_bf16_f32 v51, v70, v71\n"
    "v_cvt_pk_bf16_f32 v52, v72, v73\n"
    "v_cvt_pk_bf16_f32 v53, v74, v75\n"
    "v_cvt_pk_bf16_f32 v54, v76, v77\n"
    "v_cvt_pk_bf16_f32 v55, v78, v79\n"
    "ds_write_b64 v24, v[48:49] offset:0\n"
    "ds_write_b64 v25, v[50:51] offset:0\n"
    "ds_write_b64 v26, v[52:53] offset:0\n"
    "ds_write_b64 v27, v[54:55] offset:0\n"
    "v_pk_fma_f32 v[80:81], v[32:33], v[18:19], v[80:81] op_sel_hi:[1,0,1]\n"
    "v_pk_fma_f32 v[82:83], v[34:35], v[18:19], v[82:83] op_sel_hi:[1,0,1]\n"
    "v_pk_fma_f32 v[84:85], v[36:37], v[18:19], v[84:85] op_sel_hi:[1,0,1]\n"
    "v_pk_fma_f32 v[86:87], v[38:39], v[18:19], v[86:87] op_sel_hi:[1,0,1]\n"
    "v_pk_fma_f32 v[88:89], v[40:41], v[18:19], v[88:89] op_sel_hi:[1,0,1]\n"
    "v_pk_fma_f32 v[90:91], v[42:43], v[18:19], v[90:91] op_sel_hi:[1,0,1]\n"
    "v_pk_fma_f32 v[92:93], v[44:45], v[18:19], v[92:93] op_sel_hi:[1,0,1]\n"
    "v_pk_fma_f32 v[94:95], v[46:47], v[18:19], v[94:95] op_sel_hi:[1,0,1]\n"
    "v_pk_mul_f32 v[80:81], v[80:81], v[18:19] op_sel:[0,1] op_sel_hi:[1,1]\n"
    "v_pk_mul_f32 v[82:83], v[82:83], v[18:19] op_sel:[0,1] op_sel_hi:[1,1]\n"
    "v_pk_mul_f32 v[84:85], v[84:85], v[18:19] op_sel:[0,1] op_sel_hi:[1,1]\n"
    "v_pk_mul_f32 v[86:87], v[86:87], v[18:19] op_sel:[0,1] op_sel_hi:[1,1]\n"
    "v_pk_mul_f32 v[88:89], v[88:89], v[18:19] op_sel:[0,1] op_sel_hi:[1,1]\n"
    "v_pk_mul_f32 v[90:91], v[90:91], v[18:19] op_sel:[0,1] op_sel_hi:[1,1]\n"
    "v_pk_mul_f32 v[92:93], v[92:93], v[18:19] op_sel:[0,1] op_sel_hi:[1,1]\n"
    "v_pk_mul_f32 v[94:95], v[94:95], v[18:19] op_sel:[0,1] op_sel_hi:[1,1]\n"
    "v_cvt_pk_bf16_f32 v56, v80, v81\n"
    "v_cvt_pk_bf16_f32 v57, v82, v83\n"
    "v_cvt_pk_bf16_f32 v58, v84, v85\n"
    "v_cvt_pk_bf16_f32 v59, v86, v87\n"
    "v_cvt_pk_bf16_f32 v60, v88, v89\n"
    "v_cvt_pk_bf16_f32 v61, v90, v91\n"
    "v_cvt_pk_bf16_f32 v62, v92, v93\n"
    "v_cvt_pk_bf16_f32 v63, v94, v95\n"
    "ds_write_b64 v24, v[56:57] offset:2048\n"
    "ds_write_b64 v25, v[58:59] offset:2048\n"
    "ds_write_b64 v26, v[60:61] offset:2048\n"
    "ds_write_b64 v27, v[62:63] offset:2048\n"
    "ds_read_b64_tr_b16 v[64:65], v28 offset:0\n"
    "ds_read_b64_tr_b16 v[66:67], v28 offset:512\n"
    "ds_read_b64_tr_b16 v[68:69], v29 offset:0\n"
    "ds_read_b64_tr_b16 v[70:71], v29 offset:512\n"
    "ds_read_b64_tr_b16 v[72:73], v30 offset:0\n"
    "ds_read_b64_tr_b16 v[74:75], v30 offset:512\n"
    "ds_read_b64_tr_b16 v[76:77], v31 offset:0\n"
    "ds_read_b64_tr_b16 v[78:79], v31 offset:512\n"
    "v_pk_fma_f32 v[96:97], v[32:33], v[20:21], v[96:97] op_sel_hi:[1,0,1]\n"
    "v_pk_fma_f32 v[98:99], v[34:35], v[20:21], v[98:99] op_sel_hi:[1,0,1]\n"
    "v_pk_fma_f32 v[100:101], v[36:37], v[20:21], v[100:101] op_sel_hi:[1,0,1]\n"
    "v_pk_fma_f32 v[102:103], v[38:39], v[20:21], v[102:103] op_sel_hi:[1,0,1]\n"
    "v_pk_fma_f32 v[104:105], v[40:41], v[20:21], v[104:105] op_sel_hi:[1,0,1]\n"
    "v_pk_fma_f32 v[106:107], v[42:43], v[20:21], v[106:107] op_sel_hi:[1,0,1]\n"
    "v_pk_fma_f32 v[108:109], v[44:45], v[20:21], v[108:109] op_sel_hi:[1,0,1]\n"
    "v_pk_fma_f32 v[110:111], v[46:47], v[20:21], v[110:111] op_sel_hi:[1,0,1]\n"
    "v_pk_mul_f32 v[96:97], v[96:97], v[20:21] op_sel:[0,1] op_sel_hi:[1,1]\n"
    "v_pk_mul_f32 v[98:99], v[98:99], v[20:21] op_sel:[0,1] op_sel_hi:[1,1]\n"
    "v_pk_mul_f32 v[100:101], v[100:101], v[20:21] op_sel:[0,1] op_sel_hi:[1,1]\n"
    "v_pk_mul_f32 v[102:103], v[102:103], v[20:21] op_sel:[0,1] op_sel_hi:[1,1]\n"
    "v_pk_mul_f32 v[104:105], v[104:105], v[20:21] op_sel:[0,1] op_sel_hi:[1,1]\n"
    "v_pk_mul_f32 v[106:107], v[106:107], v[20:21] op_sel:[0,1] op_sel_hi:[1,1]\n"
    "v_pk_mul_f32 v[108:109], v[108:109], v[20:21] op_sel:[0,1] op_sel_hi:[1,1]\n"
    "v_pk_mul_f32 v[110:111], v[110:111], v[20:21] op_sel:[0,1] op_sel_hi:[1,1]\n"
    "s_waitcnt lgkmcnt(6)\n"
    "global_store_dwordx4 v10, v[64:67], s[44:45] offset:0 sc1\n"
    "s_waitcnt lgkmcnt(4)\n"
    "global_store_dwordx4 v10, v[68:71], s[46:47] offset:0 sc1\n"
    "s_waitcnt lgkmcnt(2)\n"
    "global_store_dwordx4 v10, v[72:75], s[48:49] offset:0 sc1\n"
    "s_waitcnt lgkmcnt(0)\n"
    "global_store_dwordx4 v10, v[76:79], s[50:51] offset:0 sc1\n"
    "v_cvt_pk_bf16_f32 v48, v96, v97\n"
    "v_cvt_pk_bf16_f32 v49, v98, v99\n"
    "v_cvt_pk_bf16_f32 v50, v100, v101\n"
    "v_cvt_pk_bf16_f32 v51, v102, v103\n"
    "v_cvt_pk_bf16_f32 v52, v104, v105\n"
    "v_cvt_pk_bf16_f32 v53, v106, v107\n"
    "v_cvt_pk_bf16_f32 v54, v108, v109\n"
    "v_cvt_pk_bf16_f32 v55, v110, v111\n"
    "ds_write_b64 v24, v[48:49] offset:4096\n"
    "ds_write_b64 v25, v[50:51] offset:4096\n"
    "ds_write_b64 v26, v[52:53] offset:4096\n"
    "ds_write_b64 v27, v[54:55] offset:4096\n"
    "v_pk_fma_f32 v[112:113], v[32:33], v[22:23], v[112:113] op_sel_hi:[1,0,1]\n"
    "v_pk_fma_f32 v[114:115], v[34:35], v[22:23], v[114:115] op_sel_hi:[1,0,1]\n"
    "v_pk_fma_f32 v[116:117], v[36:37], v[22:23], v[116:117] op_sel_hi:[1,0,1]\n"
    "v_pk_fma_f32 v[118:119], v[38:39], v[22:23], v[118:119] op_sel_hi:[1,0,1]\n"
    "v_pk_fma_f32 v[120:121], v[40:41], v[22:23], v[120:121] op_sel_hi:[1,0,1]\n"
    "v_pk_fma_f32 v[122:123], v[42:43], v[22:23], v[122:123] op_sel_hi:[1,0,1]\n"
    "v_pk_fma_f32 v[124:125], v[44:45], v[22:23], v[124:125] op_sel_hi:[1,0,1]\n"
    "v_pk_fma_f32 v[126:127], v[46:47], v[22:23], v[126:127] op_sel_hi:[1,0,1]\n"
    "v_pk_mul_f32 v[112:113], v[112:113], v[22:23] op_sel:[0,1] op_sel_hi:[1,1]\n"
    "v_pk_mul_f32 v[114:115], v[114:115], v[22:23] op_sel:[0,1] op_sel_hi:[1,1]\n"
    "v_pk_mul_f32 v[116:117], v[116:117], v[22:23] op_sel:[0,1] op_sel_hi:[1,1]\n"
    "v_pk_mul_f32 v[118:119], v[118:119], v[22:23] op_sel:[0,1] op_sel_hi:[1,1]\n"
    "v_pk_mul_f32 v[120:121], v[120:121], v[22:23] op_sel:[0,1] op_sel_hi:[1,1]\n"
    "v_pk_mul_f32 v[122:123], v[122:123], v[22:23] op_sel:[0,1] op_sel_hi:[1,1]\n"
    "v_pk_mul_f32 v[124:125], v[124:125], v[22:23] op_sel:[0,1] op_sel_hi:[1,1]\n"
    "v_pk_mul_f32 v[126:127], v[126:127], v[22:23] op_sel:[0,1] op_sel_hi:[1,1]\n"
    "v_cvt_pk_bf16_f32 v56, v112, v113\n"
    "v_cvt_pk_bf16_f32 v57, v114, v115\n"
    "v_cvt_pk_bf16_f32 v58, v116, v117\n"
    "v_cvt_pk_bf16_f32 v59, v118, v119\n"
    "v_cvt_pk_bf16_f32 v60, v120, v121\n"
    "v_cvt_pk_bf16_f32 v61, v122, v123\n"
    "v_cvt_pk_bf16_f32 v62, v124, v125\n"
    "v_cvt_pk_bf16_f32 v63, v126, v127\n"
    "ds_write_b64 v24, v[56:57] offset:6144\n"
    "ds_write_b64 v25, v[58:59] offset:6144\n"
    "ds_write_b64 v26, v[60:61] offset:6144\n"
    "ds_write_b64 v27, v[62:63] offset:6144\n"
    "ds_read_b64_tr_b16 v[96:97], v28 offset:4096\n"
    "ds_read_b64_tr_b16 v[98:99], v28 offset:4608\n"
    "ds_read_b64_tr_b16 v[100:101], v29 offset:4096\n"
    "ds_read_b64_tr_b16 v[102:103], v29 offset:4608\n"
    "ds_read_b64_tr_b16 v[104:105], v30 offset:4096\n"
    "ds_read_b64_tr_b16 v[106:107], v30 offset:4608\n"
    "ds_read_b64_tr_b16 v[108:109], v31 offset:4096\n"
    "ds_read_b64_tr_b16 v[110:111], v31 offset:4608\n"
    "s_waitcnt lgkmcnt(6)\n"
    "global_store_dwordx4 v10, v[96:99], s[44:45] offset:2048 sc1\n"
    "s_waitcnt lgkmcnt(4)\n"
    "global_store_dwordx4 v10, v[100:103], s[46:47] offset:2048 sc1\n"
    "s_waitcnt lgkmcnt(2)\n"
    "global_store_dwordx4 v10, v[104:107], s[48:49] offset:2048 sc1\n"
    "s_waitcnt lgkmcnt(0)\n"
    "global_store_dwordx4 v10, v[108:111], s[50:51] offset:2048 sc1\n"
    "s_branch .Lep_7\n"
    ".Lep_8:\n"
    "v_pk_fma_f32 v[64:65], v[32:33], v[16:17], v[64:65] op_sel_hi:[1,0,1]\n"
    "v_pk_fma_f32 v[66:67], v[34:35], v[16:17], v[66:67] op_sel_hi:[1,0,1]\n"
    "v_pk_fma_f32 v[68:69], v[36:37], v[16:17], v[68:69] op_sel_hi:[1,0,1]\n"
    "v_pk_fma_f32 v[70:71], v[38:39], v[16:17], v[70:71] op_sel_hi:[1,0,1]\n"
    "v_pk_fma_f32 v[72:73], v[40:41], v[16:17], v[72:73] op_sel_hi:[1,0,1]\n"
    "v_pk_fma_f32 v[74:75], v[42:43], v[16:17], v[74:75] op_sel_hi:[1,0,1]\n"
    "v_pk_fma_f32 v[76:77], v[44:45], v[16:17], v[76:77] op_sel_hi:[1,0,1]\n"
    "v_pk_fma_f32 v[78:79], v[46:47], v[16:17], v[78:79] op_sel_hi:[1,0,1]\n"
    "v_pk_mul_f32 v[64:65], v[64:65], v[16:17] op_sel:[0,1] op_sel_hi:[1,1]\n"
    "v_pk_mul_f32 v[66:67], v[66:67], v[16:17] op_sel:[0,1] op_sel_hi:[1,1]\n"
    "v_pk_mul_f32 v[68:69], v[68:69], v[16:17] op_sel:[0,1] op_sel_hi:[1,1]\n"
    "v_pk_mul_f32 v[70:71], v[70:71], v[16:17] op_sel:[0,1] op_sel_hi:[1,1]\n"
    "v_pk_mul_f32 v[72:73], v[72:73], v[16:17] op_sel:[0,1] op_sel_hi:[1,1]\n"
    "v_pk_mul_f32 v[74:75], v[74:75], v[16:17] op_sel:[0,1] op_sel_hi:[1,1]\n"
    "v_pk_mul_f32 v[76:77], v[76:77], v[16:17] op_sel:[0,1] op_sel_hi:[1,1]\n"
    "v_pk_mul_f32 v[78:79], v[78:79], v[16:17] op_sel:[0,1] op_sel_hi:[1,1]\n"
    "v_pk_fma_f32 v[80:81], v[32:33], v[18:19], v[80:81] op_sel_hi:[1,0,1]\n"
    "v_pk_fma_f32 v[82:83], v[34:35], v[18:19], v[82:83] op_sel_hi:[1,0,1]\n"
    "v_pk_fma_f32 v[84:85], v[36:37], v[18:19], v[84:85] op_sel_hi:[1,0,1]\n"
    "v_pk_fma_f32 v[86:87], v[38:39], v[18:19], v[86:87] op_sel_hi:[1,0,1]\n"
    "v_pk_fma_f32 v[88:89], v[40:41], v[18:19], v[88:89] op_sel_hi:[1,0,1]\n"
    "v_pk_fma_f32 v[90:91], v[42:43], v[18:19], v[90:91] op_sel_hi:[1,0,1]\n"
    "v_pk_fma_f32 v[92:93], v[44:45], v[18:19], v[92:93] op_sel_hi:[1,0,1]\n"
    "v_pk_fma_f32 v[94:95], v[46:47], v[18:19], v[94:95] op_sel_hi:[1,0,1]\n"
    "v_pk_mul_f32 v[80:81], v[80:81], v[18:19] op_sel:[0,1] op_sel_hi:[1,1]\n"
    "v_pk_mul_f32 v[82:83], v[82:83], v[18:19] op_sel:[0,1] op_sel_hi:[1,1]\n"
    "v_pk_mul_f32 v[84:85], v[84:85], v[18:19] op_sel:[0,1] op_sel_hi:[1,1]\n"
    "v_pk_mul_f32 v[86:87], v[86:87], v[18:19] op_sel:[0,1] op_sel_hi:[1,1]\n"
    "v_pk_mul_f32 v[88:89], v[88:89], v[18:19] op_sel:[0,1] op_sel_hi:[1,1]\n"
    "v_pk_mul_f32 v[90:91], v[90:91], v[18:19] op_sel:[0,1] op_sel_hi:[1,1]\n"
    "v_pk_mul_f32 v[92:93], v[92:93], v[18:19] op_sel:[0,1] op_sel_hi:[1,1]\n"
    "v_pk_mul_f32 v[94:95], v[94:95], v[18:19] op_sel:[0,1] op_sel_hi:[1,1]\n"
    "v_pk_fma_f32 v[96:97], v[32:33], v[20:21], v[96:97] op_sel_hi:[1,0,1]\n"
    "v_pk_fma_f32 v[98:99], v[34:35], v[20:21], v[98:99] op_sel_hi:[1,0,1]\n"
    "v_pk_fma_f32 v[100:101], v[36:37], v[20:21], v[100:101] op_sel_hi:[1,0,1]\n"
    "v_pk_fma_f32 v[102:103], v[38:39], v[20:21], v[102:103] op_sel_hi:[1,0,1]\n"
    "v_pk_fma_f32 v[104:105], v[40:41], v[20:21], v[104:105] op_sel_hi:[1,0,1]\n"
    "v_pk_fma_f32 v[106:107], v[42:43], v[20:21], v[106:107] op_sel_hi:[1,0,1]\n"
    "v_pk_fma_f32 v[108:109], v[44:45], v[20:21], v[108:109] op_sel_hi:[1,0,1]\n"
    "v_pk_fma_f32 v[110:111], v[46:47], v[20:21], v[110:111] op_sel_hi:[1,0,1]\n"
    "v_pk_mul_f32 v[96:97], v[96:97], v[20:21] op_sel:[0,1] op_sel_hi:[1,1]\n"
    "v_pk_mul_f32 v[98:99], v[98:99], v[20:21] op_sel:[0,1] op_sel_hi:[1,1]\n"
    "v_pk_mul_f32 v[100:101], v[100:101], v[20:21] op_sel:[0,1] op_sel_hi:[1,1]\n"
    "v_pk_mul_f32 v[102:103], v[102:103], v[20:21] op_sel:[0,1] op_sel_hi:[1,1]\n"
    "v_pk_mul_f32 v[104:105], v[104:105], v[20:21] op_sel:[0,1] op_sel_hi:[1,1]\n"
    "v_pk_mul_f32 v[106:107], v[106:107], v[20:21] op_sel:[0,1] op_sel_hi:[1,1]\n"
    "v_pk_mul_f32 v[108:109], v[108:109], v[20:21] op_sel:[0,1] op_sel_hi:[1,1]\n"
    "v_pk_mul_f32 v[110:111], v[110:111], v[20:21] op_sel:[0,1] op_sel_hi:[1,1]\n"
    "v_pk_fma_f32 v[112:113], v[32:33], v[22:23], v[112:113] op_sel_hi:[1,0,1]\n"
    "v_pk_fma_f32 v[114:115], v[34:35], v[22:23], v[114:115] op_sel_hi:[1,0,1]\n"
    "v_pk_fma_f32 v[116:117], v[36:37], v[22:23], v[116:117] op_sel_hi:[1,0,1]\n"
    "v_pk_fma_f32 v[118:119], v[38:39], v[22:23], v[118:119] op_sel_hi:[1,0,1]\n"
    "v_pk_fma_f32 v[120:121], v[40:41], v[22:23], v[120:121] op_sel_hi:[1,0,1]\n"
    "v_pk_fma_f32 v[122:123], v[42:43], v[22:23], v[122:123] op_sel_hi:[1,0,1]\n"
    "v_pk_fma_f32 v[124:125], v[44:45], v[22:23], v[124:125] op_sel_hi:[1,0,1]\n"
    "v_pk_fma_f32 v[126:127], v[46:47], v[22:23], v[126:127] op_sel_hi:[1,0,1]\n"
    "v_pk_mul_f32 v[112:113], v[112:113], v[22:23] op_sel:[0,1] op_sel_hi:[1,1]\n"
    "v_pk_mul_f32 v[114:115], v[114:115], v[22:23] op_sel:[0,1] op_sel_hi:[1,1]\n"
    "v_pk_mul_f32 v[116:117], v[116:117], v[22:23] op_sel:[0,1] op_sel_hi:[1,1]\n"
    "v_pk_mul_f32 v[118:119], v[118:119], v[22:23] op_sel:[0,1] op_sel_hi:[1,1]\n"
    "v_pk_mul_f32 v[120:121], v[120:121], v[22:23] op_sel:[0,1] op_sel_hi:[1,1]\n"
    "v_pk_mul_f32 v[122:123], v[122:123], v[22:23] op_sel:[0,1] op_sel_hi:[1,1]\n"
    "v_pk_mul_f32 v[124:125], v[124:125], v[22:23] op_sel:[0,1] op_sel_hi:[1,1]\n"
    "v_pk_mul_f32 v[126:127], v[126:127], v[22:23] op_sel:[0,1] op_sel_hi:[1,1]\n"
    "v_mov_b32 v16, 0x40135761\n"
    "s_mov_b32 s44, 0x3dd2d3e8\n"
    "s_mov_b32 s45, 0x3dd2d3e8\n"
    "v_pk_mul_f32 v[32:33], v[64:65], v[64:65]\n"
    "v_pk_mul_f32 v[34:35], v[66:67], v[66:67]\n"
    "v_pk_mul_f32 v[36:37], v[68:69], v[68:69]\n"
    "v_pk_mul_f32 v[38:39], v[70:71], v[70:71]\n"
    "v_pk_fma_f32 v[32:33], v[32:33], s[44:45], v[16:17] op_sel_hi:[1,1,0]\n"
    "v_pk_fma_f32 v[34:35], v[34:35], s[44:45], v[16:17] op_sel_hi:[1,1,0]\n"
    "v_pk_fma_f32 v[36:37], v[36:37], s[44:45], v[16:17] op_sel_hi:[1,1,0]\n"
    "v_pk_fma_f32 v[38:39], v[38:39], s[44:45], v[16:17] op_sel_hi:[1,1,0]\n"
    "v_pk_mul_f32 v[32:33], v[32:33], v[64:65]\n"
    "v_pk_mul_f32 v[34:35], v[34:35], v[66:67]\n"
    "v_pk_mul_f32 v[36:37], v[36:37], v[68:69]\n"
    "v_pk_mul_f32 v[38:39], v[38:39], v[70:71]\n"
    "v_exp_f32 v32, v32\n"
    "v_exp_f32 v33, v33\n"
    "v_exp_f32 v34, v34\n"
    "v_exp_f32 v35, v35\n"
    "v_exp_f32 v36, v36\n"
    "v_exp_f32 v37, v37\n"
    "v_exp_f32 v38, v38\n"
    "v_exp_f32 v39, v39\n"
    "v_pk_add_f32 v[32:33], v[32:33], 1.0 op_sel_hi:[1,0]\n"
    "v_pk_add_f32 v[34:35], v[34:35], 1.0 op_sel_hi:[1,0]\n"
    "v_pk_add_f32 v[36:37], v[36:37], 1.0 op_sel_hi:[1,0]\n"
    "v_pk_add_f32 v[38:39], v[38:39], 1.0 op_sel_hi:[1,0]\n"
    "v_rcp_f32 v32, v32\n"
    "v_rcp_f32 v33, v33\n"
    "v_rcp_f32 v34, v34\n"
    "v_rcp_f32 v35, v35\n"
    "v_rcp_f32 v36, v36\n"
    "v_rcp_f32 v37, v37\n"
    "v_rcp_f32 v38, v38\n"
    "v_rcp_f32 v39, v39\n"
    "v_pk_fma_f32 v[64:65], v[64:65], v[32:33], v[64:65] neg_lo:[1,0,0] neg_hi:[1,0,0]\n"
    "v_pk_fma_f32 v[66:67], v[66:67], v[34:35], v[66:67] neg_lo:[1,0,0] neg_hi:[1,0,0]\n"
    "v_pk_fma_f32 v[68:69], v[68:69], v[36:37], v[68:69] neg_lo:[1,0,0] neg_hi:[1,0,0]\n"
    "v_pk_fma_f32 v[70:71], v[70:71], v[38:39], v[70:71] neg_lo:[1,0,0] neg_hi:[1,0,0]\n"
    "v_pk_mul_f32 v[32:33], v[72:73], v[72:73]\n"
    "v_pk_mul_f32 v[34:35], v[74:75], v[74:75]\n"
    "v_pk_mul_f32 v[36:37], v[76:77], v[76:77]\n"
    "v_pk_mul_f32 v[38:39], v[78:79], v[78:79]\n"
    "v_pk_fma_f32 v[32:33], v[32:33], s[44:45], v[16:17] op_sel_hi:[1,1,0]\n"
    "v_pk_fma_f32 v[34:35], v[34:35], s[44:45], v[16:17] op_sel_hi:[1,1,0]\n"
    "v_pk_fma_f32 v[36:37], v[36:37], s[44:45], v[16:17] op_sel_hi:[1,1,0]\n"
    "v_pk_fma_f32 v[38:39], v[38:39], s[44:45], v[16:17] op_sel_hi:[1,1,0]\n"
    "v_pk_mul_f32 v[32:33], v[32:33], v[72:73]\n"
    "v_pk_mul_f32 v[34:35], v[34:35], v[74:75]\n"
    "v_pk_mul_f32 v[36:37], v[36:37], v[76:77]\n"
    "v_pk_mul_f32 v[38:39], v[38:39], v[78:79]\n"
    "v_exp_f32 v32, v32\n"
    "v_exp_f32 v33, v33\n"
    "v_exp_f32 v34, v34\n"
    "v_exp_f32 v35, v35\n"
    "v_exp_f32 v36, v36\n"
    "v_exp_f32 v37, v37\n"
    "v_exp_f32 v38, v38\n"
    "v_exp_f32 v39, v39\n"
    "v_pk_add_f32 v[32:33], v[32:33], 1.0 op_sel_hi:[1,0]\n"
    "v_pk_add_f32 v[34:35], v[34:35], 1.0 op_sel_hi:[1,0]\n"
    "v_pk_add_f32 v[36:37], v[36:37], 1.0 op_sel_hi:[1,0]\n"
    "v_pk_add_f32 v[38:39], v[38:39], 1.0 op_sel_hi:[1,0]\n"
    "v_rcp_f32 v32, v32\n"
    "v_rcp_f32 v33, v33\n"
    "v_rcp_f32 v34, v34\n"
    "v_rcp_f32 v35, v35\n"
    "v_rcp_f32 v36, v36\n"
    "v_rcp_f32 v37, v37\n"
    "v_rcp_f32 v38, v38\n"
    "v_rcp_f32 v39, v39\n"
    "v_pk_fma_f32 v[72:73], v[72:73], v[32:33], v[72:73] neg_lo:[1,0,0] neg_hi:[1,0,0]\n"
    "v_pk_fma_f32 v[74:75], v[74:75], v[34:35], v[74:75] neg_lo:[1,0,0] neg_hi:[1,0,0]\n"
    "v_pk_fma_f32 v[76:77], v[76:77], v[36:37], v[76:77] neg_lo:[1,0,0] neg_hi:[1,0,0]\n"
    "v_pk_fma_f32 v[78:79], v[78:79], v[38:39], v[78:79] neg_lo:[1,0,0] neg_hi:[1,0,0]\n"
    "v_pk_mul_f32 v[32:33], v[80:81], v[80:81]\n"
    "v_pk_mul_f32 v[34:35], v[82:83], v[82:83]\n"
    "v_pk_mul_f32 v[36:37], v[84:85], v[84:85]\n"
    "v_pk_mul_f32 v[38:39], v[86:87], v[86:87]\n"
    "v_pk_fma_f32 v[32:33], v[32:33], s[44:45], v[16:17] op_sel_hi:[1,1,0]\n"
    "v_pk_fma_f32 v[34:35], v[34:35], s[44:45], v[16:17] op_sel_hi:[1,1,0]\n"
    "v_pk_fma_f32 v[36:37], v[36:37], s[44:45], v[16:17] op_sel_hi:[1,1,0]\n"
    "v_pk_fma_f32 v[38:39], v[38:39], s[44:45], v[16:17] op_sel_hi:[1,1,0]\n"
    "v_pk_mul_f32 v[32:33], v[32:33], v[80:81]\n"
    "v_pk_mul_f32 v[34:35], v[34:35], v[82:83]\n"
    "v_pk_mul_f32 v[36:37], v[36:37], v[84:85]\n"
    "v_pk_mul_f32 v[38:39], v[38:39], v[86:87]\n"
    "v_exp_f32 v32, v32\n"
    "v_exp_f32 v33, v33\n"
    "v_exp_f32 v34, v34\n"
    "v_exp_f32 v35, v35\n"
    "v_exp_f32 v36, v36\n"
    "v_exp_f32 v37, v37\n"
    "v_exp_f32 v38, v38\n"
    "v_exp_f32 v39, v39\n"
    "v_pk_add_f32 v[32:33], v[32:33], 1.0 op_sel_hi:[1,0]\n"
    "v_pk_add_f32 v[34:35], v[34:35], 1.0 op_sel_hi:[1,0]\n"
    "v_pk_add_f32 v[36:37], v[36:37], 1.0 op_sel_hi:[1,0]\n"
    "v_pk_add_f32 v[38:39], v[38:39], 1.0 op_sel_hi:[1,0]\n"
    "v_rcp_f32 v32, v32\n"
    "v_rcp_f32 v33, v33\n"
    "v_rcp_f32 v34, v34\n"
    "v_rcp_f32 v35, v35\n"
    "v_rcp_f32 v36, v36\n"
    "v_rcp_f32 v37, v37\n"
    "v_rcp_f32 v38, v38\n"
    "v_rcp_f32 v39, v39\n"
    "v_pk_fma_f32 v[80:81], v[80:81], v[32:33], v[80:81] neg_lo:[1,0,0] neg_hi:[1,0,0]\n"
    "v_pk_fma_f32 v[82:83], v[82:83], v[34:35], v[82:83] neg_lo:[1,0,0] neg_hi:[1,0,0]\n"
    "v_pk_fma_f32 v[84:85], v[84:85], v[36:37], v[84:85] neg_lo:[1,0,0] neg_hi:[1,0,0]\n"
    "v_pk_fma_f32 v[86:87], v[86:87], v[38:39], v[86:87] neg_lo:[1,0,0] neg_hi:[1,0,0]\n"
    "v_pk_mul_f32 v[32:33], v[88:89], v[88:89]\n"
    "v_pk_mul_f32 v[34:35], v[90:91], v[90:91]\n"
    "v_pk_mul_f32 v[36:37], v[92:93], v[92:93]\n"
    "v_pk_mul_f32 v[38:39], v[94:95], v[94:95]\n"
    "v_pk_fma_f32 v[32:33], v[32:33], s[44:45], v[16:17] op_sel_hi:[1,1,0]\n"
    "v_pk_fma_f32 v[34:35], v[34:35], s[44:45], v[16:17] op_sel_hi:[1,1,0]\n"
    "v_pk_fma_f32 v[36:37], v[36:37], s[44:45], v[16:17] op_sel_hi:[1,1,0]\n"
    "v_pk_fma_f32 v[38:39], v[38:39], s[44:45], v[16:17] op_sel_hi:[1,1,0]\n"
    "v_pk_mul_f32 v[32:33], v[32:33], v[88:89]\n"
    "v_pk_mul_f32 v[34:35], v[34:35], v[90:91]\n"
    "v_pk_mul_f32 v[36:37], v[36:37], v[92:93]\n"
    "v_pk_mul_f32 v[38:39], v[38:39], v[94:95]\n"
    "v_exp_f32 v32, v32\n"
    "v_exp_f32 v33, v33\n"
    "v_exp_f32 v34, v34\n"
    "v_exp_f32 v35, v35\n"
    "v_exp_f32 v36, v36\n"
    "v_exp_f32 v37, v37\n"
    "v_exp_f32 v38, v38\n"
    "v_exp_f32 v39, v39\n"
    "v_pk_add_f32 v[32:33], v[32:33], 1.0 op_sel_hi:[1,0]\n"
    "v_pk_add_f32 v[34:35], v[34:35], 1.0 op_sel_hi:[1,0]\n"
    "v_pk_add_f32 v[36:37], v[36:37], 1.0 op_sel_hi:[1,0]\n"
    "v_pk_add_f32 v[38:39], v[38:39], 1.0 op_sel_hi:[1,0]\n"
    "v_rcp_f32 v32, v32\n"
    "v_rcp_f32 v33, v33\n"
    "v_rcp_f32 v34, v34\n"
    "v_rcp_f32 v35, v35\n"
    "v_rcp_f32 v36, v36\n"
    "v_rcp_f32 v37, v37\n"
    "v_rcp_f32 v38, v38\n"
    "v_rcp_f32 v39, v39\n"
    "v_pk_fma_f32 v[88:89], v[88:89], v[32:33], v[88:89] neg_lo:[1,0,0] neg_hi:[1,0,0]\n"
    "v_pk_fma_f32 v[90:91], v[90:91], v[34:35], v[90:91] neg_lo:[1,0,0] neg_hi:[1,0,0]\n"
    "v_pk_fma_f32 v[92:93], v[92:93], v[36:37], v[92:93] neg_lo:[1,0,0] neg_hi:[1,0,0]\n"
    "v_pk_fma_f32 v[94:95], v[94:95], v[38:39], v[94:95] neg_lo:[1,0,0] neg_hi:[1,0,0]\n"
    "v_pk_mul_f32 v[32:33], v[96:97], v[96:97]\n"
    "v_pk_mul_f32 v[34:35], v[98:99], v[98:99]\n"
    "v_pk_mul_f32 v[36:37], v[100:101], v[100:101]\n"
    "v_pk_mul_f32 v[38:39], v[102:103], v[102:103]\n"
    "v_pk_fma_f32 v[32:33], v[32:33], s[44:45], v[16:17] op_sel_hi:[1,1,0]\n"
    "v_pk_fma_f32 v[34:35], v[34:35], s[44:45], v[16:17] op_sel_hi:[1,1,0]\n"
    "v_pk_fma_f32 v[36:37], v[36:37], s[44:45], v[16:17] op_sel_hi:[1,1,0]\n"
    "v_pk_fma_f32 v[38:39], v[38:39], s[44:45], v[16:17] op_sel_hi:[1,1,0]\n"
    "v_pk_mul_f32 v[32:33], v[32:33], v[96:97]\n"
    "v_pk_mul_f32 v[34:35], v[34:35], v[98:99]\n"
    "v_pk_mul_f32 v[36:37], v[36:37], v[100:101]\n"
    "v_pk_mul_f32 v[38:39], v[38:39], v[102:103]\n"
    "v_exp_f32 v32, v32\n"
    "v_exp_f32 v33, v33\n"
    "v_exp_f32 v34, v34\n"
    "v_exp_f32 v35, v35\n"
    "v_exp_f32 v36, v36\n"
    "v_exp_f32 v37, v37\n"
    "v_exp_f32 v38, v38\n"
    "v_exp_f32 v39, v39\n"
    "v_pk_add_f32 v[32:33], v[32:33], 1.0 op_sel_hi:[1,0]\n"
    "v_pk_add_f32 v[34:35], v[34:35], 1.0 op_sel_hi:[1,0]\n"
    "v_pk_add_f32 v[36:37], v[36:37], 1.0 op_sel_hi:[1,0]\n"
    "v_pk_add_f32 v[38:39], v[38:39], 1.0 op_sel_hi:[1,0]\n"
    "v_rcp_f32 v32, v32\n"
    "v_rcp_f32 v33, v33\n"
    "v_rcp_f32 v34, v34\n"
    "v_rcp_f32 v35, v35\n"
    "v_rcp_f32 v36, v36\n"
    "v_rcp_f32 v37, v37\n"
    "v_rcp_f32 v38, v38\n"
    "v_rcp_f32 v39, v39\n"
    "v_pk_fma_f32 v[96:97], v[96:97], v[32:33], v[96:97] neg_lo:[1,0,0] neg_hi:[1,0,0]\n"
    "v_pk_fma_f32 v[98:99], v[98:99], v[34:35], v[98:99] neg_lo:[1,0,0] neg_hi:[1,0,0]\n"
    "v_pk_fma_f32 v[100:101], v[100:101], v[36:37], v[100:101] neg_lo:[1,0,0] neg_hi:[1,0,0]\n"
    "v_pk_fma_f32 v[102:103], v[102:103], v[38:39], v[102:103] neg_lo:[1,0,0] neg_hi:[1,0,0]\n"
    "v_pk_mul_f32 v[32:33], v[104:105], v[104:105]\n"
    "v_pk_mul_f32 v[34:35], v[106:107], v[106:107]\n"
    "v_pk_mul_f32 v[36:37], v[108:109], v[108:109]\n"
    "v_pk_mul_f32 v[38:39], v[110:111], v[110:111]\n"
    "v_pk_fma_f32 v[32:33], v[32:33], s[44:45], v[16:17] op_sel_hi:[1,1,0]\n"
    "v_pk_fma_f32 v[34:35], v[34:35], s[44:45], v[16:17] op_sel_hi:[1,1,0]\n"
    "v_pk_fma_f32 v[36:37], v[36:37], s[44:45], v[16:17] op_sel_hi:[1,1,0]\n"
    "v_pk_fma_f32 v[38:39], v[38:39], s[44:45], v[16:17] op_sel_hi:[1,1,0]\n"
    "v_pk_mul_f32 v[32:33], v[32:33], v[104:105]\n"
    "v_pk_mul_f32 v[34:35], v[34:35], v[106:107]\n"
    "v_pk_mul_f32 v[36:37], v[36:37], v[108:109]\n"
    "v_pk_mul_f32 v[38:39], v[38:39], v[110:111]\n"
    "v_exp_f32 v32, v32\n"
    "v_exp_f32 v33, v33\n"
    "v_exp_f32 v34, v34\n"
    "v_exp_f32 v35, v35\n"
    "v_exp_f32 v36, v36\n"
    "v_exp_f32 v37, v37\n"
    "v_exp_f32 v38, v38\n"
    "v_exp_f32 v39, v39\n"
    "v_pk_add_f32 v[32:33], v[32:33], 1.0 op_sel_hi:[1,0]\n"
    "v_pk_add_f32 v[34:35], v[34:35], 1.0 op_sel_hi:[1,0]\n"
    "v_pk_add_f32 v[36:37], v[36:37], 1.0 op_sel_hi:[1,0]\n"
    "v_pk_add_f32 v[38:39], v[38:39], 1.0 op_sel_hi:[1,0]\n"
    "v_rcp_f32 v32, v32\n"
    "v_rcp_f32 v33, v33\n"
    "v_rcp_f32 v34, v34\n"
    "v_rcp_f32 v35, v35\n"
    "v_rcp_f32 v36, v36\n"
    "v_rcp_f32 v37, v37\n"
    "v_rcp_f32 v38, v38\n"
    "v_rcp_f32 v39, v39\n"
    "v_pk_fma_f32 v[104:105], v[104:105], v[32:33], v[104:105] neg_lo:[1,0,0] neg_hi:[1,0,0]\n"
    "v_pk_fma_f32 v[106:107], v[106:107], v[34:35], v[106:107] neg_lo:[1,0,0] neg_hi:[1,0,0]\n"
    "v_pk_fma_f32 v[108:109], v[108:109], v[36:37], v[108:109] neg_lo:[1,0,0] neg_hi:[1,0,0]\n"
    "v_pk_fma_f32 v[110:111], v[110:111], v[38:39], v[110:111] neg_lo:[1,0,0] neg_hi:[1,0,0]\n"
    "v_pk_mul_f32 v[32:33], v[112:113], v[112:113]\n"
    "v_pk_mul_f32 v[34:35], v[114:115], v[114:115]\n"
    "v_pk_mul_f32 v[36:37], v[116:117], v[116:117]\n"
    "v_pk_mul_f32 v[38:39], v[118:119], v[118:119]\n"
    "v_pk_fma_f32 v[32:33], v[32:33], s[44:45], v[16:17] op_sel_hi:[1,1,0]\n"
    "v_pk_fma_f32 v[34:35], v[34:35], s[44:45], v[16:17] op_sel_hi:[1,1,0]\n"
    "v_pk_fma_f32 v[36:37], v[36:37], s[44:45], v[16:17] op_sel_hi:[1,1,0]\n"
    "v_pk_fma_f32 v[38:39], v[38:39], s[44:45], v[16:17] op_sel_hi:[1,1,0]\n"
    "v_pk_mul_f32 v[32:33], v[32:33], v[112:113]\n"
    "v_pk_mul_f32 v[34:35], v[34:35], v[114:115]\n"
    "v_pk_mul_f32 v[36:37], v[36:37], v[116:117]\n"
    "v_pk_mul_f32 v[38:39], v[38:39], v[118:119]\n"
    "v_exp_f32 v32, v32\n"
    "v_exp_f32 v33, v33\n"
    "v_exp_f32 v34, v34\n"
    "v_exp_f32 v35, v35\n"
    "v_exp_f32 v36, v36\n"
    "v_exp_f32 v37, v37\n"
    "v_exp_f32 v38, v38\n"
    "v_exp_f32 v39, v39\n"
    "v_pk_add_f32 v[32:33], v[32:33], 1.0 op_sel_hi:[1,0]\n"
    "v_pk_add_f32 v[34:35], v[34:35], 1.0 op_sel_hi:[1,0]\n"
    "v_pk_add_f32 v[36:37], v[36:37], 1.0 op_sel_hi:[1,0]\n"
    "v_pk_add_f32 v[38:39], v[38:39], 1.0 op_sel_hi:[1,0]\n"
    "v_rcp_f32 v32, v32\n"
    "v_rcp_f32 v33, v33\n"
    "v_rcp_f32 v34, v34\n"
    "v_rcp_f32 v35, v35\n"
    "v_rcp_f32 v36, v36\n"
    "v_rcp_f32 v37, v37\n"
    "v_rcp_f32 v38, v38\n"
    "v_rcp_f32 v39, v39\n"
    "v_pk_fma_f32 v[112:113], v[112:113], v[32:33], v[112:113] neg_lo:[1,0,0] neg_hi:[1,0,0]\n"
    "v_pk_fma_f32 v[114:115], v[114:115], v[34:35], v[114:115] neg_lo:[1,0,0] neg_hi:[1,0,0]\n"
    "v_pk_fma_f32 v[116:117], v[116:117], v[36:37], v[116:117] neg_lo:[1,0,0] neg_hi:[1,0,0]\n"
    "v_pk_fma_f32 v[118:119], v[118:119], v[38:39], v[118:119] neg_lo:[1,0,0] neg_hi:[1,0,0]\n"
    "v_pk_mul_f32 v[32:33], v[120:121], v[120:121]\n"
    "v_pk_mul_f32 v[34:35], v[122:123], v[122:123]\n"
    "v_pk_mul_f32 v[36:37], v[124:125], v[124:125]\n"
    "v_pk_mul_f32 v[38:39], v[126:127], v[126:127]\n"
    "v_pk_fma_f32 v[32:33], v[32:33], s[44:45], v[16:17] op_sel_hi:[1,1,0]\n"
    "v_pk_fma_f32 v[34:35], v[34:35], s[44:45], v[16:17] op_sel_hi:[1,1,0]\n"
    "v_pk_fma_f32 v[36:37], v[36:37], s[44:45], v[16:17] op_sel_hi:[1,1,0]\n"
    "v_pk_fma_f32 v[38:39], v[38:39], s[44:45], v[16:17] op_sel_hi:[1,1,0]\n"
    "v_pk_mul_f32 v[32:33], v[32:33], v[120:121]\n"
    "v_pk_mul_f32 v[34:35], v[34:35], v[122:123]\n"
    "v_pk_mul_f32 v[36:37], v[36:37], v[124:125]\n"
    "v_pk_mul_f32 v[38:39], v[38:39], v[126:127]\n"
    "v_exp_f32 v32, v32\n"
    "v_exp_f32 v33, v33\n"
    "v_exp_f32 v34, v34\n"
    "v_exp_f32 v35, v35\n"
    "v_exp_f32 v36, v36\n"
    "v_exp_f32 v37, v37\n"
    "v_exp_f32 v38, v38\n"
    "v_exp_f32 v39, v39\n"
    "v_pk_add_f32 v[32:33], v[32:33], 1.0 op_sel_hi:[1,0]\n"
    "v_pk_add_f32 v[34:35], v[34:35], 1.0 op_sel_hi:[1,0]\n"
    "v_pk_add_f32 v[36:37], v[36:37], 1.0 op_sel_hi:[1,0]\n"
    "v_pk_add_f32 v[38:39], v[38:39], 1.0 op_sel_hi:[1,0]\n"
    "v_rcp_f32 v32, v32\n"
    "v_rcp_f32 v33, v33\n"
    "v_rcp_f32 v34, v34\n"
    "v_rcp_f32 v35, v35\n"
    "v_rcp_f32 v36, v36\n"
    "v_rcp_f32 v37, v37\n"
    "v_rcp_f32 v38, v38\n"
    "v_rcp_f32 v39, v39\n"
    "v_pk_fma_f32 v[120:121], v[120:121], v[32:33], v[120:121] neg_lo:[1,0,0] neg_hi:[1,0,0]\n"
    "v_pk_fma_f32 v[122:123], v[122:123], v[34:35], v[122:123] neg_lo:[1,0,0] neg_hi:[1,0,0]\n"
    "v_pk_fma_f32 v[124:125], v[124:125], v[36:37], v[124:125] neg_lo:[1,0,0] neg_hi:[1,0,0]\n"
    "v_pk_fma_f32 v[126:127], v[126:127], v[38:39], v[126:127] neg_lo:[1,0,0] neg_hi:[1,0,0]\n"
    "s_cmp_eq_u32 s39, 1\n"
    "s_cbranch_scc0 .Lep_5\n"
    "ds_read_b32 v17, v13 offset:72\n"
    "ds_read_b32 v19, v13 offset:76\n"
    "v_pk_mul_f32 v[34:35], v[64:65], v[64:65]\n"
    "v_pk_mul_f32 v[38:39], v[80:81], v[80:81]\n"
    "v_pk_mul_f32 v[42:43], v[96:97], v[96:97]\n"
    "v_pk_mul_f32 v[46:47], v[112:113], v[112:113]\n"
    "v_pk_add_f32 v[32:33], v[64:65], v[66:67]\n"
    "v_pk_fma_f32 v[34:35], v[66:67], v[66:67], v[34:35]\n"
    "v_pk_add_f32 v[36:37], v[80:81], v[82:83]\n"
    "v_pk_fma_f32 v[38:39], v[82:83], v[82:83], v[38:39]\n"
    "v_pk_add_f32 v[40:41], v[96:97], v[98:99]\n"
    "v_pk_fma_f32 v[42:43], v[98:99], v[98:99], v[42:43]\n"
    "v_pk_add_f32 v[44:45], v[112:113], v[114:115]\n"
    "v_pk_fma_f32 v[46:47], v[114:115], v[114:115], v[46:47]\n"
    "v_pk_add_f32 v[32:33], v[32:33], v[68:69]\n"
    "v_pk_fma_f32 v[34:35], v[68:69], v[68:69], v[34:35]\n"
    "v_pk_add_f32 v[36:37], v[36:37], v[84:85]\n"
    "v_pk_fma_f32 v[38:39], v[84:85], v[84:85], v[38:39]\n"
    "v_pk_add_f32 v[40:41], v[40:41], v[100:101]\n"
    "v_pk_fma_f32 v[42:43], v[100:101], v[100:101], v[42:43]\n"
    "v_pk_add_f32 v[44:45], v[44:45], v[116:117]\n"
    "v_pk_fma_f32 v[46:47], v[116:117], v[116:117], v[46:47]\n"
    "v_pk_add_f32 v[32:33], v[32:33], v[70:71]\n"
    "v_pk_fma_f32 v[34:35], v[70:71], v[70:71], v[34:35]\n"
    "v_pk_add_f32 v[36:37], v[36:37], v[86:87]\n"
    "v_pk_fma_f32 v[38:39], v[86:87], v[86:87], v[38:39]\n"
    "v_pk_add_f32 v[40:41], v[40:41], v[102:103]\n"
    "v_pk_fma_f32 v[42:43], v[102:103], v[102:103], v[42:43]\n"
    "v_pk_add_f32 v[44:45], v[44:45], v[118:119]\n"
    "v_pk_fma_f32 v[46:47], v[118:119], v[118:119], v[46:47]\n"
    "v_pk_add_f32 v[32:33], v[32:33], v[72:73]\n"
    "v_pk_fma_f32 v[34:35], v[72:73], v[72:73], v[34:35]\n"
    "v_pk_add_f32 v[36:37], v[36:37], v[88:89]\n"
    "v_pk_fma_f32 v[38:39], v[88:89], v[88:89], v[38:39]\n"
    "v_pk_add_f32 v[40:41], v[40:41], v[104:105]\n"
    "v_pk_fma_f32 v[42:43], v[104:105], v[104:105], v[42:43]\n"
    "v_pk_add_f32 v[44:45], v[44:45], v[120:121]\n"
    "v_pk_fma_f32 v[46:47], v[120:121], v[120:121], v[46:47]\n"
    "v_pk_add_f32 v[32:33], v[32:33], v[74:75]\n"
    "v_pk_fma_f32 v[34:35], v[74:75], v[74:75], v[34:35]\n"
    "v_pk_add_f32 v[36:37], v[36:37], v[90:91]\n"
    "v_pk_fma_f32 v[38:39], v[90:91], v[90:91], v[38:39]\n"
    "v_pk_add_f32 v[40:41], v[40:41], v[106:107]\n"
    "v_pk_fma_f32 v[42:43], v[106:107], v[106:107], v[42:43]\n"
    "v_pk_add_f32 v[44:45], v[44:45], v[122:123]\n"
    "v_pk_fma_f32 v[46:47], v[122:123], v[122:123], v[46:47]\n"
    "v_pk_add_f32 v[32:33], v[32:33], v[76:77]\n"
    "v_pk_fma_f32 v[34:35], v[76:77], v[76:77], v[34:35]\n"
    "v_pk_add_f32 v[36:37], v[36:37], v[92:93]\n"
    "v_pk_fma_f32 v[38:39], v[92:93], v[92:93], v[38:39]\n"
    "v_pk_add_f32 v[40:41], v[40:41], v[108:109]\n"
    "v_pk_fma_f32 v[42:43], v[108:109], v[108:109], v[42:43]\n"
    "v_pk_add_f32 v[44:45], v[44:45], v[124:125]\n"
    "v_pk_fma_f32 v[46:47], v[124:125], v[124:125], v[46:47]\n"
    "v_pk_add_f32 v[32:33], v[32:33], v[78:79]\n"
    "v_pk_fma_f32 v[34:35], v[78:79], v[78:79], v[34:35]\n"
    "v_pk_add_f32 v[36:37], v[36:37], v[94:95]\n"
    "v_pk_fma_f32 v[38:39], v[94:95], v[94:95], v[38:39]\n"
    "v_pk_add_f32 v[40:41], v[40:41], v[110:111]\n"
    "v_pk_fma_f32 v[42:43], v[110:111], v[110:111], v[42:43]\n"
    "v_pk_add_f32 v[44:45], v[44:45], v[126:127]\n"
    "v_pk_fma_f32 v[46:47], v[126:127], v[126:127], v[46:47]\n"
    "v_add_f32 v32, v32, v33\n"
    "v_add_f32 v34, v34, v35\n"
    "v_add_f32 v36, v36, v37\n"
    "v_add_f32 v38, v38, v39\n"
    "v_add_f32 v40, v40, v41\n"
    "v_add_f32 v42, v42, v43\n"
    "v_add_f32 v44, v44, v45\n"
    "v_add_f32 v46, v46, v47\n"
    "s_nop 1\n"
    "v_permlane16_swap_b32 v32, v36\n"
    "v_permlane16_swap_b32 v40, v44\n"
    "v_permlane16_swap_b32 v34, v38\n"
    "v_permlane16_swap_b32 v42, v46\n"
    "v_add_f32 v32, v32, v36\n"
    "v_add_f32 v40, v40, v44\n"
    "v_add_f32 v34, v34, v38\n"
    "v_add_f32 v42, v42, v46\n"
    "s_nop 1\n"
    "v_permlane32_swap_b32 v32, v40\n"
    "v_permlane32_swap_b32 v34, v42\n"
    "v_add_f32 v16, v32, v40\n"
    "v_add_f32 v18, v34, v42\n"
    "s_waitcnt lgkmcnt(0)\n"
    "global_store_dwordx4 v10, v[16:19], s[42:43] sc1\n"
    "v_cvt_pk_bf16_f32 v32, v64, v65\n"
    "v_cvt_pk_bf16_f32 v33, v66, v67\n"
    "v_cvt_pk_bf16_f32 v34, v68, v69\n"
    "v_cvt_pk_bf16_f32 v35, v70, v71\n"
    "v_cvt_pk_bf16_f32 v36, v72, v73\n"
    "v_cvt_pk_bf16_f32 v37, v74, v75\n"
    "v_cvt_pk_bf16_f32 v38, v76, v77\n"
    "v_cvt_pk_bf16_f32 v39, v78, v79\n"
    "ds_write_b64 v24, v[32:33] offset:0\n"
    "ds_write_b64 v25, v[34:35] offset:0\n"
    "ds_write_b64 v26, v[36:37] offset:0\n"
    "ds_write_b64 v27, v[38:39] offset:0\n"
    "v_cvt_pk_bf16_f32 v40, v80, v81\n"
    "v_cvt_pk_bf16_f32 v41, v82, v83\n"
    "v_cvt_pk_bf16_f32 v42, v84, v85\n"
    "v_cvt_pk_bf16_f32 v43, v86, v87\n"
    "v_cvt_pk_bf16_f32 v44, v88, v89\n"
    "v_cvt_pk_bf16_f32 v45, v90, v91\n"
    "v_cvt_pk_bf16_f32 v46, v92, v93\n"
    "v_cvt_pk_bf16_f32 v47, v94, v95\n"
    "ds_write_b64 v24, v[40:41] offset:2048\n"
    "ds_write_b64 v25, v[42:43] offset:2048\n"
    "ds_write_b64 v26, v[44:45] offset:2048\n"
    "ds_write_b64 v27, v[46:47] offset:2048\n"
    "v_cvt_pk_bf16_f32 v48, v96, v97\n"
    "v_cvt_pk_bf16_f32 v49, v98, v99\n"
    "v_cvt_pk_bf16_f32 v50, v100, v101\n"
    "v_cvt_pk_bf16_f32 v51, v102, v103\n"
    "v_cvt_pk_bf16_f32 v52, v104, v105\n"
    "v_cvt_pk_bf16_f32 v53, v106, v107\n"
    "v_cvt_pk_bf16_f32 v54, v108, v109\n"
    "v_cvt_pk_bf16_f32 v55, v110, v111\n"
    "ds_write_b64 v24, v[48:49] offset:4096\n"
    "ds_write_b64 v25, v[50:51] offset:4096\n"
    "ds_write_b64 v26, v[52:53] offset:4096\n"
    "ds_write_b64 v27, v[54:55] offset:4096\n"
    "v_cvt_pk_bf16_f32 v56, v112, v113\n"
    "v_cvt_pk_bf16_f32 v57, v114, v115\n"
    "v_cvt_pk_bf16_f32 v58, v116, v117\n"
    "v_cvt_pk_bf16_f32 v59, v118, v119\n"
    "v_cvt_pk_bf16_f32 v60, v120, v121\n"
    "v_cvt_pk_bf16_f32 v61, v122, v123\n"
    "v_cvt_pk_bf16_f32 v62, v124, v125\n"
    "v_cvt_pk_bf16_f32 v63, v126, v127\n"
    "ds_write_b64 v24, v[56:57] offset:6144\n"
    "ds_write_b64 v25, v[58:59] offset:6144\n"
    "ds_write_b64 v26, v[60:61] offset:6144\n"
    "ds_write_b64 v27, v[62:63] offset:6144\n"
    "s_branch .Lep_7\n"
    ".Lep_5:\n"
    "v_cvt_pk_bf16_f32 v32, v64, v65\n"
    "v_cvt_pk_bf16_f32 v33, v66, v67\n"
    "v_cvt_pk_bf16_f32 v34, v68, v69\n"
    "v_cvt_pk_bf16_f32 v35, v70, v71\n"
    "v_cvt_pk_bf16_f32 v36, v72, v73\n"
    "v_cvt_pk_bf16_f32 v37, v74, v75\n"
    "v_cvt_pk_bf16_f32 v38, v76, v77\n"
    "v_cvt_pk_bf16_f32 v39, v78, v79\n"
    "v_permlane16_swap_b32 v32, v34\n"
    "v_permlane16_swap_b32 v33, v35\n"
    "v_permlane16_swap_b32 v36, v38\n"
    "v_permlane16_swap_b32 v37, v39\n"
    "ds_write_b128 v10, v[32:35] offset:0\n"
    "ds_write_b128 v10, v[36:39] offset:4096\n"
    "v_cvt_pk_bf16_f32 v40, v80, v81\n"
    "v_cvt_pk_bf16_f32 v41, v82, v83\n"
    "v_cvt_pk_bf16_f32 v42, v84, v85\n"
    "v_cvt_pk_bf16_f32 v43, v86, v87\n"
    "v_cvt_pk_bf16_f32 v44, v88, v89\n"
    "v_cvt_pk_bf16_f32 v45, v90, v91\n"
    "v_cvt_pk_bf16_f32 v46, v92, v93\n"
    "v_cvt_pk_bf16_f32 v47, v94, v95\n"
    "v_permlane16_swap_b32 v40, v42\n"
    "v_permlane16_swap_b32 v41, v43\n"
    "v_permlane16_swap_b32 v44, v46\n"
    "v_permlane16_swap_b32 v45, v47\n"
    "ds_write_b128 v10, v[40:43] offset:1024\n"
    "ds_write_b128 v10, v[44:47] offset:5120\n"
    "v_cvt_pk_bf16_f32 v48, v96, v97\n"
    "v_cvt_pk_bf16_f32 v49, v98, v99\n"
    "v_cvt_pk_bf16_f32 v50, v100, v101\n"
    "v_cvt_pk_bf16_f32 v51, v102, v103\n"
    "v_cvt_pk_bf16_f32 v52, v104, v105\n"
    "v_cvt_pk_bf16_f32 v53, v106, v107\n"
    "v_cvt_pk_bf16_f32 v54, v108, v109\n"
    "v_cvt_pk_bf16_f32 v55, v110, v111\n"
    "v_permlane16_swap_b32 v48, v50\n"
    "v_permlane16_swap_b32 v49, v51\n"
    "v_permlane16_swap_b32 v52, v54\n"
    "v_permlane16_swap_b32 v53, v55\n"
    "ds_write_b128 v10, v[48:51] offset:2048\n"
    "ds_write_b128 v10, v[52:55] offset:6144\n"
    "v_cvt_pk_bf16_f32 v56, v112, v113\n"
    "v_cvt_pk_bf16_f32 v57, v114, v115\n"
    "v_cvt_pk_bf16_f32 v58, v116, v117\n"
    "v_cvt_pk_bf16_f32 v59, v118, v119\n"
    "v_cvt_pk_bf16_f32 v60, v120, v121\n"
    "v_cvt_pk_bf16_f32 v61, v122, v123\n"
    "v_cvt_pk_bf16_f32 v62, v124, v125\n"
    "v_cvt_pk_bf16_f32 v63, v126, v127\n"
    "v_permlane16_swap_b32 v56, v58\n"
    "v_permlane16_swap_b32 v57, v59\n"
    "v_permlane16_swap_b32 v60, v62\n"
    "v_permlane16_swap_b32 v61, v63\n"
    "ds_write_b128 v10, v[56:59] offset:3072\n"
    "ds_write_b128 v10, v[60:63] offset:7168\n"
    ".Lep_7:\n"
    "s_nop 1\n"
    "v_readfirstlane_b32 s34, v14\n"
    "v_readfirstlane_b32 s35, v15\n"
    "s_cmp_lt_u32 s39, 2\n"
    "s_cbranch_scc1 .Lzarr\n"
    "s_lshl_b32 s41, s55, 6\n"
    "s_add_u32 s41, s41, s56\n"
    "s_lshl_b32 s41, s41, 6\n"
    "s_add_u32 s41, s41, 23592960\n"
    "v_mov_b32 v2, s34\n"
    "v_mov_b32 v3, s35\n"
    "v_lshl_add_u32 v4, v0, 3, s41\n"
    "s_waitcnt vmcnt(0)\n"
    "s_mov_b64 exec, 0xffff\n"
    "global_store_dwordx2 v4, v[2:3], s[10:11] sc1\n"
    "s_mov_b64 exec, -1\n"
    "s_waitcnt lgkmcnt(0)\n"
    "s_mov_b64 exec, 1\n"
    "v_mov_b32 v1, 1\n"
    "ds_add_u32 v13, v1 offset:48\n"
    "s_mov_b64 exec, -1\n"
    "s_branch .Lattn\n"
    ".Lzarr:\n"
    "s_lshl2_add_u32 s41, s54, 56\n"
    "s_cmp_eq_u32 s39, 0\n"
    "s_cselect_b32 s41, 104, s41\n"
    "v_add_u32 v2, s41, v13\n"
    "s_waitcnt lgkmcnt(0)\n"
    "s_mov_b64 exec, 1\n"
    "v_mov_b32 v1, 1\n"
    "ds_add_u32 v2, v1 offset:0\n"
    "s_mov_b64 exec, -1\n"
    ".Lattn:\n"
    "s_and_b32 s52, s26, 3\n"
    "s_lshl2_add_u32 s52, s25, s52\n"
    "s_lshr_b32 s55, s24, 2\n"
    "s_and_b32 s42, s24, 3\n"
    "s_and_b32 s53, s26, 28\n"
    "s_add_u32 s53, s53, s42\n"
    "s_lshl1_add_u32 s53, s53, s55\n"
    "s_lshr_b32 s41, s52, 1\n"
    "s_lshl4_add_u32 s41, s53, s41\n"
    "s_and_b32 s40, s52, 1\n"
    "s_lshl2_add_u32 s41, s41, s40\n"
    "s_lshl_b32 s41, s41, 9\n"
    "s_xor_b32 s40, s41, 0x8000\n"
    "s_add_u32 s44, s10, s41\n"
    "s_addc_u32 s45, s11, 0\n"
    "s_add_u32 s46, s10, s40\n"
    "s_addc_u32 s47, s11, 0\n"
    "v_mov_b32 v119, v13\n"
    "s_lshl_b32 s42, s42, 2\n"
    "v_add_u32 v120, s42, v13\n"
    "v_lshlrev_b32 v116, 4, v0\n"
    "v_and_b32 v123, 0xf0, v116\n"
    "v_and_b32 v1, 16, v0\n"
    "v_lshl_or_b32 v123, v1, 6, v123\n"
    "v_and_b32 v1, 32, v0\n"
    "v_lshl_or_b32 v123, v1, 3, v123\n"
    "s_lshl_b32 s40, s55, 11\n"
    "v_add_u32 v116, s40, v116\n"
    "v_xor_b32 v14, 0x800, v116\n"
    "s_lshl2_add_u32 s40, s24, 157888\n"
    "v_mov_b32 v121, s40\n"
    "s_xor_b32 s40, s40, 16\n"
    "v_mov_b32 v122, s40\n"
    "s_mov_b32 s54, 0xc2b40000\n"
    "v_mov_b32 v118, 0xdddddddd\n"
    "s_mov_b32 s56, 0xffff\n"
    "s_mov_b32 s57, 0xffff\n"
    "v_and_b32 v1, 27, v0\n"
    "v_cmp_eq_u32 vcc, 16, v1\n"
    "v_mov_b32 v1, 0x3f803f80\n"
    "s_nop 0\n"
    "v_cndmask_b32 v108, 0, v1, vcc\n"
    "v_mov_b32 v109, v108\n"
    "v_mov_b32 v110, v108\n"
    "v_mov_b32 v111, v108\n"
    "v_mov_b32 v112, v108\n"
    "v_mov_b32 v113, v108\n"
    "v_mov_b32 v114, v108\n"
    "v_mov_b32 v115, v108\n"
    "v_mov_b64 v[16:17], 0\n"
    "v_mov_b64 v[18:19], 0\n"
    "v_mov_b64 v[20:21], 0\n"
    "v_mov_b64 v[22:23], 0\n"
    "v_mov_b64 v[24:25], 0\n"
    "v_mov_b64 v[26:27], 0\n"
    "v_mov_b64 v[28:29], 0\n"
    "v_mov_b64 v[30:31], 0\n"
    "s_lshl_b32 s48, s53, 5\n"
    "s_add_u32 s48, s48, s52\n"
    "s_lshl_b32 s48, s48, 7\n"
    "s_add_u32 s48, s48, 23965696\n"
    ".Lw_1:\n"
    "ds_read_b32 v117, v120 offset:80\n"
    "s_waitcnt lgkmcnt(0)\n"
    "v_readfirstlane_b32 s60, v117\n"
    "s_cmp_ge_u32 s60, 1\n"
    "s_cbranch_scc1 .Ld_2\n"
    "s_sleep 1\n"
    "s_add_u32 s63, s63, 1\n"
    "s_bitcmp1_b32 s63, 19\n"
    "s_cbranch_scc0 .Lw_1\n"
    "s_sub_u32 s63, s63, 1\n"
    ".Ld_2:\n"
    "global_load_dwordx4 v[96:99], v123, s[44:45] sc1\n"
    "global_load_dwordx4 v[100:103], v123, s[46:47] sc1\n"
    "s_mov_b32 s36, 0\n"
    "s_mov_b32 s37, 6\n"
    "s_setprio 1\n"
    ".Lat_4:\n"
    "ds_read_b32 v117, v119\n"
    "s_waitcnt lgkmcnt(0)\n"
    "v_readfirstlane_b32 s38, v117\n"
    "s_cmp_ge_u32 s38, 2\n"
    "s_cbranch_scc0 .Lat_4\n"
    "ds_read_b128 v[104:107], v116\n"
    "ds_read_b128 v[124:127], v14\n"
    "s_mov_b64 exec, s[56:57]\n"
    "ds_read_b128 v[108:111], v116 offset:1024\n"
    "s_mov_b64 exec, -1\n"
    "s_waitcnt vmcnt(0) lgkmcnt(2)\n"
    "v_mfma_f32_32x32x16_bf16 v[32:47], v[104:107], v[96:99], 0\n"
    "s_waitcnt lgkmcnt(1)\n"
    "v_mfma_f32_32x32x16_bf16 v[64:79], v[124:127], v[96:99], 0\n"
    "v_mfma_f32_32x32x16_bf16 v[48:63], v[104:107], v[100:103], 0\n"
    "v_mfma_f32_32x32x16_bf16 v[80:95], v[124:127], v[100:103], 0\n"
    "ds_read_b32 v117, v119\n"
    "s_nop 6\n"
    "v_max3_f32 v0, v32, v33, v34\n"
    "v_max3_f32 v1, v35, v36, v37\n"
    "v_max3_f32 v2, v38, v39, v40\n"
    "v_max3_f32 v0, v0, v41, v42\n"
    "v_max3_f32 v1, v1, v43, v44\n"
    "v_max3_f32 v2, v2, v45, v46\n"
    "v_max3_f32 v0, v0, v47, v64\n"
    "v_max3_f32 v1, v1, v65, v66\n"
    "v_max3_f32 v2, v2, v67, v68\n"
    "v_max3_f32 v0, v0, v69, v70\n"
    "v_max3_f32 v1, v1, v71, v72\n"
    "v_max3_f32 v2, v2, v73, v74\n"
    "v_max3_f32 v0, v0, v75, v76\n"
    "v_max3_f32 v1, v1, v77, v78\n"
    "v_max_f32 v2, v2, v79\n"
    "v_max3_f32 v0, v0, v1, v2\n"
    "v_mov_b32 v1, v0\n"
    "s_nop 1\n"
    "v_permlane32_swap_b32 v0, v1\n"
    "v_max_f32 v0, v0, v1\n"
    "v_sub_f32 v64, s54, v0\n"
    "v_sub_f32 v65, s54, v0\n"
    "v_mov_b64 v[66:67], v[64:65]\n"
    "v_mov_b64 v[68:69], v[64:65]\n"
    "v_mov_b64 v[70:71], v[64:65]\n"
    "v_mov_b64 v[72:73], v[64:65]\n"
    "v_mov_b64 v[74:75], v[64:65]\n"
    "v_mov_b64 v[76:77], v[64:65]\n"
    "v_mov_b64 v[78:79], v[64:65]\n"
    "s_waitcnt lgkmcnt(0)\n"
    "v_readfirstlane_b32 s38, v117\n"
    "v_mfma_f32_32x32x16_bf16 v[32:47], v[104:107], v[96:99], v[64:79]\n"
    "v_max3_f32 v4, v48, v49, v50\n"
    "v_max3_f32 v5, v51, v52, v53\n"
    "v_max3_f32 v6, v54, v55, v56\n"
    "v_max3_f32 v4, v4, v57, v58\n"
    "v_max3_f32 v5, v5, v59, v60\n"
    "v_max3_f32 v6, v6, v61, v62\n"
    "v_max3_f32 v4, v4, v63, v80\n"
    "v_max3_f32 v5, v5, v81, v82\n"
    "v_max3_f32 v6, v6, v83, v84\n"
    "v_max3_f32 v4, v4, v85, v86\n"
    "v_max3_f32 v5, v5, v87, v88\n"
    "v_max3_f32 v6, v6, v89, v90\n"
    "v_max3_f32 v4, v4, v91, v92\n"
    "v_max3_f32 v5, v5, v93, v94\n"
    "v_max_f32 v6, v6, v95\n"
    "v_max3_f32 v4, v4, v5, v6\n"
    "v_mov_b32 v5, v4\n"
    "s_nop 1\n"
    "v_permlane32_swap_b32 v4, v5\n"
    "v_max_f32 v4, v4, v5\n"
    "v_sub_f32 v80, s54, v4\n"
    "v_sub_f32 v81, s54, v4\n"
    "v_mov_b64 v[82:83], v[80:81]\n"
    "v_mov_b64 v[84:85], v[80:81]\n"
    "v_mov_b64 v[86:87], v[80:81]\n"
    "v_mov_b64 v[88:89], v[80:81]\n"
    "v_mov_b64 v[90:91], v[80:81]\n"
    "v_mov_b64 v[92:93], v[80:81]\n"
    "v_mov_b64 v[94:95], v[80:81]\n"
    "v_mov_b64 v[56:57], 0\n"
    "v_mov_b64 v[58:59], 0\n"
    "v_mov_b64 v[60:61], 0\n"
    "v_mov_b64 v[62:63], 0\n"
    "v_mov_b64 v[0:1], 0\n"
    "v_mov_b64 v[2:3], 0\n"
    "v_mov_b64 v[4:5], 0\n"
    "v_mov_b64 v[6:7], 0\n"
    "v_mov_b64 v[8:9], 0\n"
    "v_mov_b64 v[10:11], 0\n"
    "v_mov_b64 v[12:13], 0\n"
    "v_mov_b64 v[14:15], 0\n"
    "v_mov_b32 v124, -1\n"
    "v_mov_b32 v125, -1\n"
    "v_mov_b32 v126, -1\n"
    "v_mov_b32 v127, -1\n"
    "s_cmp_ge_u32 s38, s37\n"
    "s_cbranch_scc0 .Lat_6\n"
    ".Lat_1:\n"
    "v_exp_f32 v32, v32\n"
    "v_exp_f32 v33, v33\n"
    "s_setprio 3\n"
    "v_smfmac_f32_32x32x32_bf16 v[16:31], v[112:115], v[56:63], v118\n"
    "s_setprio 1\n"
    "s_mov_b64 exec, s[56:57]\n"
    "ds_read_b128 v[112:115], v116 offset:1280\n"
    "s_mov_b64 exec, -1\n"
    "v_exp_f32 v34, v34\n"
    "v_exp_f32 v35, v35\n"
    "v_exp_f32 v36, v36\n"
    "v_exp_f32 v37, v37\n"
    "v_exp_f32 v38, v38\n"
    "s_setprio 3\n"
    "v_mfma_f32_32x32x16_bf16 v[48:63], v[104:107], v[100:103], v[80:95]\n"
    "s_setprio 1\n"
    "ds_read_b128 v[104:107], v116 offset:4096\n"
    "v_exp_f32 v39, v39\n"
    "v_exp_f32 v40, v40\n"
    "v_exp_f32 v41, v41\n"
    "v_exp_f32 v42, v42\n"
    "v_exp_f32 v43, v43\n"
    "s_setprio 3\n"
    "s_waitcnt lgkmcnt(2)\n"
    "v_smfmac_f32_32x32x32_bf16 v[0:15], v[108:111], v[32:39], v118\n"
    "s_setprio 1\n"
    "v_exp_f32 v44, v44\n"
    "v_exp_f32 v45, v45\n"
    "v_exp_f32 v46, v46\n"
    "v_exp_f32 v47, v47\n"
    "v_exp_f32 v48, v48\n"
    "v_exp_f32 v49, v49\n"
    "s_setprio 3\n"
    "s_waitcnt lgkmcnt(1)\n"
    "v_smfmac_f32_32x32x32_bf16 v[0:15], v[112:115], v[40:47], v118\n"
    "s_setprio 1\n"
    "v_exp_f32 v50, v50\n"
    "v_exp_f32 v51, v51\n"
    "v_exp_f32 v52, v52\n"
    "v_exp_f32 v53, v53\n"
    "v_exp_f32 v54, v54\n"
    "s_setprio 3\n"
    "s_waitcnt lgkmcnt(0)\n"
    "v_mfma_f32_32x32x16_bf16 v[32:47], v[104:107], v[96:99], v[64:79]\n"
    "s_setprio 1\n"
    "v_exp_f32 v55, v55\n"
    "v_exp_f32 v56, v56\n"
    "v_exp_f32 v57, v57\n"
    "v_exp_f32 v58, v58\n"
    "v_exp_f32 v59, v59\n"
    "s_setprio 3\n"
    "v_smfmac_f32_32x32x32_bf16 v[16:31], v[108:111], v[48:55], v118\n"
    "s_setprio 1\n"
    "s_mov_b64 exec, s[56:57]\n"
    "ds_read_b128 v[108:111], v116 offset:5120\n"
    "s_mov_b64 exec, -1\n"
    "v_exp_f32 v60, v60\n"
    "v_exp_f32 v61, v61\n"
    "v_exp_f32 v62, v62\n"
    "v_exp_f32 v63, v63\n"
    "v_exp_f32 v32, v32\n"
    "v_exp_f32 v33, v33\n"
    "s_setprio 3\n"
    "v_smfmac_f32_32x32x32_bf16 v[16:31], v[112:115], v[56:63], v118\n"
    "s_setprio 1\n"
    "s_mov_b64 exec, s[56:57]\n"
    "ds_read_b128 v[112:115], v116 offset:5376\n"
    "s_mov_b64 exec, -1\n"
    "v_exp_f32 v34, v34\n"
    "v_exp_f32 v35, v35\n"
    "v_exp_f32 v36, v36\n"
    "v_exp_f32 v37, v37\n"
    "v_exp_f32 v38, v38\n"
    "s_setprio 3\n"
    "v_mfma_f32_32x32x16_bf16 v[48:63], v[104:107], v[100:103], v[80:95]\n"
    "s_setprio 1\n"
    "ds_read_b128 v[104:107], v116 offset:8192\n"
    "v_exp_f32 v39, v39\n"
    "v_exp_f32 v40, v40\n"
    "v_exp_f32 v41, v41\n"
    "v_exp_f32 v42, v42\n"
    "v_exp_f32 v43, v43\n"
    "s_setprio 3\n"
    "s_waitcnt lgkmcnt(2)\n"
    "v_smfmac_f32_32x32x32_bf16 v[0:15], v[108:111], v[32:39], v118\n"
    "s_setprio 1\n"
    "ds_read_b32 v117, v119\n"
    "ds_read_b32 v120, v122\n"
    "v_exp_f32 v44, v44\n"
    "v_exp_f32 v45, v45\n"
    "v_exp_f32 v46, v46\n"
    "v_exp_f32 v47, v47\n"
    "v_exp_f32 v48, v48\n"
    "v_exp_f32 v49, v49\n"
    "s_setprio 3\n"
    "s_waitcnt lgkmcnt(3)\n"
    "v_smfmac_f32_32x32x32_bf16 v[0:15], v[112:115], v[40:47], v118\n"
    "s_setprio 1\n"
    "v_exp_f32 v50, v50\n"
    "s_add_u32 s36, s36, 1\n"
    "v_exp_f32 v51, v51\n"
    "s_add_u32 s37, s37, 4\n"
    "v_exp_f32 v52, v52\n"
    "s_min_u32 s37, s37, 64\n"
    "v_exp_f32 v53, v53\n"
    "s_bitcmp1_b32 0x11100, s36\n"
    "v_exp_f32 v54, v54\n"
    "s_setprio 3\n"
    "s_waitcnt lgkmcnt(0)\n"
    "v_mfma_f32_32x32x16_bf16 v[32:47], v[104:107], v[96:99], v[64:79]\n"
    "s_setprio 1\n"
    "v_readfirstlane_b32 s38, v117\n"
    "s_cselect_b32 s39, 0x100, s37\n"
    "v_readfirstlane_b32 s58, v120\n"
    "v_exp_f32 v55, v55\n"
    "v_exp_f32 v56, v56\n"
    "v_exp_f32 v57, v57\n"
    "v_exp_f32 v58, v58\n"
    "v_exp_f32 v59, v59\n"
    "s_setprio 3\n"
    "v_smfmac_f32_32x32x32_bf16 v[16:31], v[108:111], v[48:55], v118\n"
    "s_setprio 1\n"
    "s_mov_b64 exec, s[56:57]\n"
    "ds_read_b128 v[108:111], v116 offset:9216\n"
    "s_mov_b64 exec, -1\n"
    "v_add_u32 v116, 0x2000, v116\n"
    "s_mov_b64 exec, 1\n"
    "ds_write_b32 v121, v116\n"
    "s_mov_b64 exec, -1\n"
    "s_lshl_b32 s59, s36, 13\n"
    "s_cmp_ge_u32 s38, s39\n"
    "v_exp_f32 v60, v60\n"
    "v_exp_f32 v61, v61\n"
    "v_exp_f32 v62, v62\n"
    "v_exp_f32 v63, v63\n"
    "s_cbranch_scc0 .Lat_ev\n"
    "s_cmp_le_u32 s59, s58\n"
    "s_cbranch_scc1 .Lat_1\n"
    ".Lat_lo:\n"
    "v_exp_f32 v32, v32\n"
    "v_exp_f32 v33, v33\n"
    "s_setprio 3\n"
    "v_smfmac_f32_32x32x32_bf16 v[16:31], v[112:115], v[56:63], v118\n"
    "s_setprio 0\n"
    "s_mov_b64 exec, s[56:57]\n"
    "ds_read_b128 v[112:115], v116 offset:1280\n"
    "s_mov_b64 exec, -1\n"
    "v_exp_f32 v34, v34\n"
    "v_exp_f32 v35, v35\n"
    "v_exp_f32 v36, v36\n"
    "v_exp_f32 v37, v37\n"
    "v_exp_f32 v38, v38\n"
    "s_setprio 3\n"
    "v_mfma_f32_32x32x16_bf16 v[48:63], v[104:107], v[100:103], v[80:95]\n"
    "s_setprio 0\n"
    "ds_read_b128 v[104:107], v116 offset:4096\n"
    "v_exp_f32 v39, v39\n"
    "v_exp_f32 v40, v40\n"
    "v_exp_f32 v41, v41\n"
    "v_exp_f32 v42, v42\n"
    "v_exp_f32 v43, v43\n"
    "s_setprio 3\n"
    "s_waitcnt lgkmcnt(2)\n"
    "v_smfmac_f32_32x32x32_bf16 v[0:15], v[108:111], v[32:39], v118\n"
    "s_setprio 0\n"
    "v_exp_f32 v44, v44\n"
    "v_exp_f32 v45, v45\n"
    "v_exp_f32 v46, v46\n"
    "v_exp_f32 v47, v47\n"
    "v_exp_f32 v48, v48\n"
    "v_exp_f32 v49, v49\n"
    "s_setprio 3\n"
    "s_waitcnt lgkmcnt(1)\n"
    "v_smfmac_f32_32x32x32_bf16 v[0:15], v[112:115], v[40:47], v118\n"
    "s_setprio 0\n"
    "v_exp_f32 v50, v50\n"
    "v_exp_f32 v51, v51\n"
    "v_exp_f32 v52, v52\n"
    "v_exp_f32 v53, v53\n"
    "v_exp_f32 v54, v54\n"
    "s_setprio 3\n"
    "s_waitcnt lgkmcnt(0)\n"
    "v_mfma_f32_32x32x16_bf16 v[32:47], v[104:107], v[96:99], v[64:79]\n"
    "s_setprio 0\n"
    "v_exp_f32 v55, v55\n"
    "v_exp_f32 v56, v56\n"
    "v_exp_f32 v57, v57\n"
    "v_exp_f32 v58, v58\n"
    "v_exp_f32 v59, v59\n"
    "s_setprio 3\n"
    "v_smfmac_f32_32x32x32_bf16 v[16:31], v[108:111], v[48:55], v118\n"
    "s_setprio 0\n"
    "s_mov_b64 exec, s[56:57]\n"
    "ds_read_b128 v[108:111], v116 offset:5120\n"
    "s_mov_b64 exec, -1\n"
    "v_exp_f32 v60, v60\n"
    "v_exp_f32 v61, v61\n"
    "v_exp_f32 v62, v62\n"
    "v_exp_f32 v63, v63\n"
    "v_exp_f32 v32, v32\n"
    "v_exp_f32 v33, v33\n"
    "s_setprio 3\n"
    "v_smfmac_f32_32x32x32_bf16 v[16:31], v[112:115], v[56:63], v118\n"
    "s_setprio 0\n"
    "s_mov_b64 exec, s[56:57]\n"
    "ds_read_b128 v[112:115], v116 offset:5376\n"
    "s_mov_b64 exec, -1\n"
    "v_exp_f32 v34, v34\n"
    "v_exp_f32 v35, v35\n"
    "v_exp_f32 v36, v36\n"
    "v_exp_f32 v37, v37\n"
    "v_exp_f32 v38, v38\n"
    "s_setprio 3\n"
    "v_mfma_f32_32x32x16_bf16 v[48:63], v[104:107], v[100:103], v[80:95]\n"
    "s_setprio 0\n"
    "ds_read_b128 v[104:107], v116 offset:8192\n"
    "v_exp_f32 v39, v39\n"
    "v_exp_f32 v40, v40\n"
    "v_exp_f32 v41, v41\n"
    "v_exp_f32 v42, v42\n"
    "v_exp_f32 v43, v43\n"
    "s_setprio 3\n"
    "s_waitcnt lgkmcnt(2)\n"
    "v_smfmac_f32_32x32x32_bf16 v[0:15], v[108:111], v[32:39], v118\n"
    "s_setprio 0\n"
    "ds_read_b32 v117, v119\n"
    "ds_read_b32 v120, v122\n"
    "v_exp_f32 v44, v44\n"
    "v_exp_f32 v45, v45\n"
    "v_exp_f32 v46, v46\n"
    "v_exp_f32 v47, v47\n"
    "v_exp_f32 v48, v48\n"
    "v_exp_f32 v49, v49\n"
    "s_setprio 3\n"
    "s_waitcnt lgkmcnt(3)\n"
    "v_smfmac_f32_32x32x32_bf16 v[0:15], v[112:115], v[40:47], v118\n"
    "s_setprio 0\n"
    "v_exp_f32 v50, v50\n"
    "s_add_u32 s36, s36, 1\n"
    "v_exp_f32 v51, v51\n"
    "s_add_u32 s37, s37, 4\n"
    "v_exp_f32 v52, v52\n"
    "s_min_u32 s37, s37, 64\n"
    "v_exp_f32 v53, v53\n"
    "s_bitcmp1_b32 0x11100, s36\n"
    "v_exp_f32 v54, v54\n"
    "s_setprio 3\n"
    "s_waitcnt lgkmcnt(0)\n"
    "v_mfma_f32_32x32x16_bf16 v[32:47], v[104:107], v[96:99], v[64:79]\n"
    "s_setprio 0\n"
    "v_readfirstlane_b32 s38, v117\n"
    "s_cselect_b32 s39, 0x100, s37\n"
    "v_readfirstlane_b32 s58, v120\n"
    "v_exp_f32 v55, v55\n"
    "v_exp_f32 v56, v56\n"
    "v_exp_f32 v57, v57\n"
    "v_exp_f32 v58, v58\n"
    "v_exp_f32 v59, v59\n"
    "s_setprio 3\n"
    "v_smfmac_f32_32x32x32_bf16 v[16:31], v[108:111], v[48:55], v118\n"
    "s_setprio 0\n"
    "s_mov_b64 exec, s[56:57]\n"
    "ds_read_b128 v[108:111], v116 offset:9216\n"
    "s_mov_b64 exec, -1\n"
    "v_add_u32 v116, 0x2000, v116\n"
    "s_mov_b64 exec, 1\n"
    "ds_write_b32 v121, v116\n"
    "s_mov_b64 exec, -1\n"
    "s_lshl_b32 s59, s36, 13\n"
    "s_cmp_ge_u32 s38, s39\n"
    "v_exp_f32 v60, v60\n"
    "v_exp_f32 v61, v61\n"
    "v_exp_f32 v62, v62\n"
    "v_exp_f32 v63, v63\n"
    "s_cbranch_scc0 .Lat_ev\n"
    "s_cmp_le_u32 s59, s58\n"
    "s_cbranch_scc1 .Lat_1\n"
    "s_branch .Lat_lo\n"
    ".Lat_ev:\n"
    "s_cmp_eq_u32 s36, 16\n"
    "s_cbranch_scc1 .Lat_9\n"
    "s_cmp_eq_u32 s36, 8\n"
    "s_cbranch_scc0 .Lat_5\n"
    "s_cmp_ge_u32 s58, 1\n"
    "s_cbranch_scc1 .Lat_7\n"
    ".Lw_3:\n"
    "ds_read_b32 v117, v122 offset:0\n"
    "s_waitcnt lgkmcnt(0)\n"
    "v_readfirstlane_b32 s60, v117\n"
    "s_cmp_ge_u32 s60, 1\n"
    "s_cbranch_scc1 .Ld_4\n"
    "s_sleep 1\n"
    "s_add_u32 s63, s63, 1\n"
    "s_bitcmp1_b32 s63, 19\n"
    "s_cbranch_scc0 .Lw_3\n"
    "s_sub_u32 s63, s63, 1\n"
    ".Ld_4:\n"
    ".Lat_7:\n"
    "global_store_dwordx4 v123, v[124:127], s[44:45] sc1\n"
    ".Lat_5:\n"
    "s_cmp_eq_u32 s36, 12\n"
    "s_cbranch_scc0 .Lat_2\n"
    "s_mov_b64 exec, 1\n"
    "v_mov_b32 v117, 1\n"
    "ds_add_u32 v119, v117 offset:116\n"
    "s_waitcnt vmcnt(0)\n"
    "v_mov_b32 v124, s34\n"
    "v_mov_b32 v125, s35\n"
    "v_mov_b32 v126, s48\n"
    "global_store_dwordx2 v126, v[124:125], s[10:11] sc1\n"
    "s_mov_b64 exec, -1\n"
    ".Lat_2:\n"
    "s_cmp_ge_u32 s38, s37\n"
    "s_cbranch_scc1 .Lat_1\n"
    ".Lat_6:\n"
    "ds_read_b32 v117, v119\n"
    "s_waitcnt lgkmcnt(0)\n"
    "v_readfirstlane_b32 s38, v117\n"
    "s_cmp_ge_u32 s38, s37\n"
    "s_cbranch_scc0 .Lat_6\n"
    "s_branch .Lat_1\n"
    ".Lat_9:\n"
    "s_setprio 3\n"
    "v_smfmac_f32_32x32x32_bf16 v[16:31], v[112:115], v[56:63], v118\n"
    "v_mbcnt_lo_u32_b32 v116, -1, 0\n"
    "v_mbcnt_hi_u32_b32 v116, -1, v116\n"
    "v_and_b32 v117, 7, v116\n"
    "v_lshl_add_u32 v120, v117, 2, v119\n"
    "s_mul_i32 s40, s24, 2304\n"
    "s_add_u32 s40, s40, 98304\n"
    "s_xor_b32 s41, s24, 4\n"
    "s_mul_i32 s41, s41, 2304\n"
    "s_add_u32 s41, s41, 98304\n"
    "v_lshl_add_u32 v124, v116, 4, s40\n"
    "v_lshl_add_u32 v125, v116, 2, s40\n"
    "v_lshl_add_u32 v126, v116, 4, s41\n"
    "v_lshl_add_u32 v127, v116, 2, s41\n"
    ".Lx_1:\n"
    "ds_read_b32 v117, v120 offset:192\n"
    "s_waitcnt lgkmcnt(0)\n"
    "v_cmp_gt_u32 vcc, 122880, v117\n"
    "s_cmp_eq_u64 vcc, 0\n"
    "s_cbranch_scc1 .Lx_2\n"
    "s_sleep 1\n"
    "s_branch .Lx_1\n"
    ".Lx_2:\n"
    "ds_write_b128 v124, v[16:19]\n"
    "ds_write_b128 v124, v[20:23] offset:1024\n"
    "ds_write_b32 v125, v24 offset:2048\n"
    "s_mov_b64 exec, 1\n"
    "ds_write_b32 v121, v118 offset:32\n"
    "s_mov_b64 exec, -1\n"
    ".Lx_3:\n"
    "ds_read_b32 v117, v122 offset:32\n"
    "ds_read_b128 v[32:35], v126\n"
    "ds_read_b128 v[36:39], v126 offset:1024\n"
    "ds_read_b32 v40, v127 offset:2048\n"
    "s_waitcnt lgkmcnt(0)\n"
    "v_readfirstlane_b32 s40, v117\n"
    "s_cmp_lg_u32 s40, 0\n"
    "s_cbranch_scc1 .Lx_4\n"
    "s_sleep 1\n"
    "s_branch .Lx_3\n"
    ".Lx_4:\n"
    "v_pk_add_f32 v[0:1], v[0:1], v[32:33]\n"
    "v_pk_add_f32 v[2:3], v[2:3], v[34:35]\n"
    "v_pk_add_f32 v[4:5], v[4:5], v[36:37]\n"
    "v_pk_add_f32 v[6:7], v[6:7], v[38:39]\n"
    "v_add_f32 v8, v8, v40\n"
    ".Lchk:\n"
    "v_rcp_f32 v16, v8\n"
    "v_max3_f32 v18, |v0|, |v1|, |v2|\n"
    "v_add_f32_e64 v19, v8, |v0|\n"
    "v_max3_f32 v18, v18, |v3|, |v4|\n"
    "v_add_f32_e64 v19, v19, |v1|\n"
    "v_add_f32_e64 v19, v19, |v2|\n"
    "v_add_f32_e64 v19, v19, |v3|\n"
    "v_add_f32_e64 v19, v19, |v4|\n"
    "v_add_f32_e64 v19, v19, |v5|\n"
    "v_add_f32_e64 v19, v19, |v6|\n"
    "v_add_f32_e64 v19, v19, |v7|\n"
    "v_max3_f32 v18, v18, |v5|, |v6|\n"
    "v_max_f32_e64 v18, v18, |v7|\n"
    "v_cmp_gt_f32 vcc, 0x7cf0bdc2, v19\n"
    "s_mov_b64 s[40:41], vcc\n"
    "v_cmp_lt_f32 vcc, 0x08a6274c, v18\n"
    "v_pk_mul_f32 v[0:1], v[0:1], v[16:17] op_sel_hi:[1,0]\n"
    "v_pk_mul_f32 v[2:3], v[2:3], v[16:17] op_sel_hi:[1,0]\n"
    "v_pk_mul_f32 v[4:5], v[4:5], v[16:17] op_sel_hi:[1,0]\n"
    "v_pk_mul_f32 v[6:7], v[6:7], v[16:17] op_sel_hi:[1,0]\n"
    "s_and_b64 s[40:41], s[40:41], vcc\n"
    "v_cvt_pk_bf16_f32 v20, v0, v1\n"
    "v_cvt_pk_bf16_f32 v21, v2, v3\n"
    "v_cvt_pk_bf16_f32 v22, v4, v5\n"
    "v_cvt_pk_bf16_f32 v23, v6, v7\n"
    "s_cmp_eq_u32 s54, 0\n"
    "s_cselect_b64 s[40:41], -1, s[40:41]\n"
    "v_min_u32 v20, -2, v20\n"
    "v_min_u32 v21, -2, v21\n"
    "v_min_u32 v22, -2, v22\n"
    "v_min_u32 v23, -2, v23\n"
    "s_cmp_eq_u64 s[40:41], -1\n"
    "s_cbranch_scc0 .Lfallback\n"
    "global_store_dwordx4 v123, v[20:23], s[44:45] sc1\n"
    "s_waitcnt lgkmcnt(0)\n"
    "s_mov_b64 exec, 1\n"
    "v_mov_b32 v117, 1\n"
    "ds_add_u32 v119, v117 offset:120\n"
    "s_mov_b64 exec, -1\n"
    "s_endpgm\n"
    ".Lidle:\n"
    "s_cmp_eq_u32 s24, 14\n"
    "s_cbranch_scc1 .Lgather\n"
    "s_cmp_eq_u32 s24, 15\n"
    "s_cbranch_scc1 .Lwpf\n"
    "s_barrier\n"
    "s_barrier\n"
    "s_barrier\n"
    "s_barrier\n"
    "s_barrier\n"
    "s_barrier\n"
    "s_barrier\n"
    "s_barrier\n"
    "s_barrier\n"
    "s_cmp_lt_u32 s24, 8\n"
    "s_cbranch_scc0 .Lph2\n"
    "s_waitcnt lgkmcnt(0)\n"
    "s_mov_b64 exec, 1\n"
    "v_mov_b32 v1, 1\n"
    "ds_add_u32 v13, v1 offset:48\n"
    "s_mov_b64 exec, -1\n"
    "ds_read_b64 v[2:3], v13 offset:72\n"
    "s_waitcnt lgkmcnt(0)\n"
    "v_readfirstlane_b32 s34, v2\n"
    "v_readfirstlane_b32 s35, v3\n"
    "s_nop 1\n"
    "s_branch .Lattn\n"
    ".Lwpf:\n"
    "s_lshl2_add_u32 s40, s25, s27\n"
    "s_lshl_b32 s40, s40, 18\n"
    "s_waitcnt lgkmcnt(0)\n"
    "s_add_u32 s42, s16, s40\n"
    "s_addc_u32 s43, s17, 0\n"
    "s_mul_i32 s60, s30, 28\n"
    "s_add_u32 s60, s60, 64\n"
    "v_and_b32 v7, 31, v0\n"
    "v_min_u32 v7, 27, v7\n"
    "v_add_u32 v7, s60, v7\n"
    "v_lshrrev_b32 v8, 5, v0\n"
    "s_lshl_b32 s60, s31, 7\n"
    "v_mul_u32_u24 v9, 0xe00, v7\n"
    "v_mul_u32_u24 v10, 1792, v8\n"
    "v_add3_u32 v9, v9, v10, s60\n"
    "s_and_b32 s60, s31, 1\n"
    "s_lshl1_add_u32 s60, s30, s60\n"
    "s_lshl_b32 s60, s60, 16\n"
    "s_lshr_b32 s61, s31, 1\n"
    "s_lshl_b32 s61, s61, 7\n"
    "s_add_u32 s60, s60, s61\n"
    "s_add_u32 s60, s60, 128\n"
    "v_lshl_add_u32 v11, v0, 10, s60\n"
    "s_min_u32 s60, s32, 22\n"
    "s_lshl_b32 s60, s60, 7\n"
    "v_mul_u32_u24 v12, 0xc00, v7\n"
    "v_lshlrev_b32 v10, 7, v8\n"
    "v_add3_u32 v12, v12, v10, s60\n"
    "s_mov_b64 s[40:41], s[42:43]\n"
    "s_lshl_b32 s62, s25, 2\n"
    "s_lshl_b32 s59, s27, 1\n"
    "s_xor_b32 s59, s59, s28\n"
    "s_lshl_b32 s59, s59, 1\n"
    "v_lshrrev_b32 v1, 3, v0\n"
    "v_and_b32 v2, 7, v0\n"
    "v_lshrrev_b32 v5, 4, v0\n"
    "v_xor_b32 v2, v2, v5\n"
    "v_lshlrev_b32 v2, 4, v2\n"
    "v_lshl_or_b32 v4, v1, 12, v2\n"
    "v_xor_b32 v5, 64, v4\n"
    "v_add_u32 v5, 0x8000, v5\n"
    "s_barrier\n"
    "global_load_dword v6, v9, s[8:9]\n"
    "global_load_dword v6, v11, s[4:5]\n"
    "global_load_dword v6, v12, s[6:7]\n"
    "s_barrier\n"
    "s_barrier\n"
    "s_barrier\n"
    "s_barrier\n"
    "s_barrier\n"
    "s_barrier\n"
    "s_xor_b32 s45, 0, s59\n"
    "s_add_u32 s45, s45, s62\n"
    "s_and_b32 s45, s45, 31\n"
    "s_lshl_b32 s45, s45, 7\n"
    "s_add_u32 m0, 0, 131072\n"
    "s_add_u32 s42, s40, s45\n"
    "s_addc_u32 s43, s41, 0\n"
    "global_load_lds_dwordx4 v4, s[42:43]\n"
    "s_add_u32 m0, m0, 0x400\n"
    "s_nop 0\n"
    "global_load_lds_dwordx4 v5, s[42:43]\n"
    "s_add_u32 m0, m0, 0x400\n"
    "s_add_u32 s42, s42, 0x10000\n"
    "s_addc_u32 s43, s43, 0\n"
    "global_load_lds_dwordx4 v4, s[42:43]\n"
    "s_add_u32 m0, m0, 0x400\n"
    "s_nop 0\n"
    "global_load_lds_dwordx4 v5, s[42:43]\n"
    "s_add_u32 m0, m0, 0x400\n"
    "s_add_u32 s42, s42, 0x10000\n"
    "s_addc_u32 s43, s43, 0\n"
    "global_load_lds_dwordx4 v4, s[42:43]\n"
    "s_add_u32 m0, m0, 0x400\n"
    "s_nop 0\n"
    "global_load_lds_dwordx4 v5, s[42:43]\n"
    "s_add_u32 m0, m0, 0x400\n"
    "s_add_u32 s42, s42, 0x10000\n"
    "s_addc_u32 s43, s43, 0\n"
    "global_load_lds_dwordx4 v4, s[42:43]\n"
    "s_add_u32 m0, m0, 0x400\n"
    "s_nop 0\n"
    "global_load_lds_dwordx4 v5, s[42:43]\n"
    "s_xor_b32 s45, 1, s59\n"
    "s_add_u32 s45, s45, s62\n"
    "s_and_b32 s45, s45, 31\n"
    "s_lshl_b32 s45, s45, 7\n"
    "s_add_u32 m0, 0, 139264\n"
    "s_add_u32 s42, s40, s45\n"
    "s_addc_u32 s43, s41, 0\n"
    "global_load_lds_dwordx4 v4, s[42:43]\n"
    "s_add_u32 m0, m0, 0x400\n"
    "s_nop 0\n"
    "global_load_lds_dwordx4 v5, s[42:43]\n"
    "s_add_u32 m0, m0, 0x400\n"
    "s_add_u32 s42, s42, 0x10000\n"
    "s_addc_u32 s43, s43, 0\n"
    "global_load_lds_dwordx4 v4, s[42:43]\n"
    "s_add_u32 m0, m0, 0x400\n"
    "s_nop 0\n"
    "global_load_lds_dwordx4 v5, s[42:43]\n"
    "s_add_u32 m0, m0, 0x400\n"
    "s_add_u32 s42, s42, 0x10000\n"
    "s_addc_u32 s43, s43, 0\n"
    "global_load_lds_dwordx4 v4, s[42:43]\n"
    "s_add_u32 m0, m0, 0x400\n"
    "s_nop 0\n"
    "global_load_lds_dwordx4 v5, s[42:43]\n"
    "s_add_u32 m0, m0, 0x400\n"
    "s_add_u32 s42, s42, 0x10000\n"
    "s_addc_u32 s43, s43, 0\n"
    "global_load_lds_dwordx4 v4, s[42:43]\n"
    "s_add_u32 m0, m0, 0x400\n"
    "s_nop 0\n"
    "global_load_lds_dwordx4 v5, s[42:43]\n"
    "s_xor_b32 s45, 2, s59\n"
    "s_add_u32 s45, s45, s62\n"
    "s_and_b32 s45, s45, 31\n"
    "s_lshl_b32 s45, s45, 7\n"
    "s_add_u32 m0, 0, 147456\n"
    "s_add_u32 s42, s40, s45\n"
    "s_addc_u32 s43, s41, 0\n"
    "global_load_lds_dwordx4 v4, s[42:43]\n"
    "s_add_u32 m0, m0, 0x400\n"
    "s_nop 0\n"
    "global_load_lds_dwordx4 v5, s[42:43]\n"
    "s_add_u32 m0, m0, 0x400\n"
    "s_add_u32 s42, s42, 0x10000\n"
    "s_addc_u32 s43, s43, 0\n"
    "global_load_lds_dwordx4 v4, s[42:43]\n"
    "s_add_u32 m0, m0, 0x400\n"
    "s_nop 0\n"
    "global_load_lds_dwordx4 v5, s[42:43]\n"
    "s_add_u32 m0, m0, 0x400\n"
    "s_add_u32 s42, s42, 0x10000\n"
    "s_addc_u32 s43, s43, 0\n"
    "global_load_lds_dwordx4 v4, s[42:43]\n"
    "s_add_u32 m0, m0, 0x400\n"
    "s_nop 0\n"
    "global_load_lds_dwordx4 v5, s[42:43]\n"
    "s_add_u32 m0, m0, 0x400\n"
    "s_add_u32 s42, s42, 0x10000\n"
    "s_addc_u32 s43, s43, 0\n"
    "global_load_lds_dwordx4 v4, s[42:43]\n"
    "s_add_u32 m0, m0, 0x400\n"
    "s_nop 0\n"
    "global_load_lds_dwordx4 v5, s[42:43]\n"
    "s_barrier\n"
    "s_barrier\n"
    "s_waitcnt vmcnt(0)\n"
    "s_waitcnt lgkmcnt(0)\n"
    "s_mov_b64 exec, 1\n"
    "v_mov_b32 v1, 1\n"
    "ds_add_u32 v13, v1 offset:152\n"
    "s_mov_b64 exec, -1\n"
    "s_branch .Lph2\n"
    ".Lgather:\n"
    "v_and_b32 v6, 3, v0\n"
    "v_lshrrev_b32 v7, 2, v0\n"
    "v_and_b32 v8, 1, v0\n"
    "v_bfe_u32 v9, v0, 1, 1\n"
    "s_min_u32 s40, s32, 22\n"
    "s_lshl_b32 s40, s40, 4\n"
    "s_add_u32 s40, s40, s30\n"
    "s_lshl_b32 s40, s40, 8\n"
    "s_add_u32 s40, s40, 23592960\n"
    "v_lshl_add_u32 v10, v8, 7, s40\n"
    "v_lshl_add_u32 v10, v9, 12, v10\n"
    "s_lshl4_add_u32 s40, s31, s30\n"
    "s_lshl_b32 s41, s40, 9\n"
    "s_add_u32 s41, s41, 23719936\n"
    "v_lshl_add_u32 v11, v6, 7, s41\n"
    "v_cmp_eq_u32 vcc, 1, v7\n"
    "s_nop 1\n"
    "v_cndmask_b32 v10, v10, v11, vcc\n"
    "s_lshl_b32 s41, s40, 11\n"
    "s_add_u32 s41, s41, 23135104\n"
    "v_lshl_add_u32 v11, v8, 10, s41\n"
    "v_cmp_eq_u32 vcc, 2, v7\n"
    "s_nop 1\n"
    "v_cndmask_b32 v10, v10, v11, vcc\n"
    "s_lshl2_add_u32 s40, s25, s27\n"
    "s_lshl3_add_u32 s40, s40, s28\n"
    "s_lshl_b32 s40, s40, 9\n"
    "s_add_u32 s40, s40, 23834624\n"
    "v_lshl_add_u32 v11, v6, 7, s40\n"
    "v_cmp_eq_u32 vcc, 3, v7\n"
    "s_nop 1\n"
    "v_cndmask_b32 v10, v10, v11, vcc\n"
    "v_mov_b32 v1, 24260608\n"
    "s_waitcnt lgkmcnt(0)\n"
    "global_load_dwordx2 v[2:3], v1, s[10:11] sc1\n"
    "v_lshl_add_u32 v4, v0, 2, v13\n"
    "v_mov_b32 v5, 0\n"
    "ds_write_b32 v4, v5\n"
    "s_mov_b64 exec, 0xffff\n"
    "global_load_dword v12, v10, s[10:11] sc1\n"
    "s_mov_b64 exec, -1\n"
    "s_waitcnt vmcnt(1)\n"
    "v_add_u32 v2, 1, v2\n"
    "v_add_u32 v3, 1, v3\n"
    "ds_write_b64 v13, v[2:3] offset:72\n"
    "v_readfirstlane_b32 s34, v2\n"
    "v_readfirstlane_b32 s35, v3\n"
    "s_waitcnt lgkmcnt(0)\n"
    "s_barrier\n"
    "s_barrier\n"
    "s_barrier\n"
    "s_barrier\n"
    "s_barrier\n"
    "s_barrier\n"
    "v_lshrrev_b32 v1, 5, v0\n"
    "v_and_b32 v2, 31, v0\n"
    "v_lshlrev_b32 v2, 4, v2\n"
    "s_add_u32 s40, s31, 24\n"
    "v_mad_u32_u24 v3, v1, 14, s40\n"
    "v_lshl_add_u32 v5, v3, 9, v2\n"
    "v_add_u32 v3, s32, v1\n"
    "v_lshl_add_u32 v6, v3, 9, v2\n"
    "s_cmp_lt_u32 s28, 6\n"
    "s_cselect_b64 vcc, -1, 0\n"
    "v_cndmask_b32 v6, v5, v6, vcc\n"
    "v_lshlrev_b32 v4, 4, v0\n"
    "s_lshl_b32 s40, s30, 11\n"
    "s_add_u32 s40, s40, 23068672\n"
    "s_add_u32 s42, s10, s40\n"
    "s_addc_u32 s43, s11, 0\n"
    "s_add_u32 s44, s10, 23101440\n"
    "s_addc_u32 s45, s11, 0\n"
    "s_mov_b32 s36, 0\n"
    "s_sleep 10\n"
    ".Lg_l:\n"
    "global_load_dwordx4 v[16:19], v4, s[42:43] sc1\n"
    "global_load_dwordx4 v[20:23], v4, s[42:43] offset:1024 sc1\n"
    "global_load_dwordx4 v[24:27], v5, s[44:45] sc1\n"
    "global_load_dwordx4 v[28:31], v6, s[44:45] sc1\n"
    "s_cmp_lt_u32 s36, 2\n"
    "s_cbranch_scc0 .Lg_n\n"
    "s_barrier\n"
    ".Lg_n:\n"
    "s_add_u32 s36, s36, 1\n"
    "s_waitcnt vmcnt(0)\n"
    "v_xor_b32 v17, s34, v17\n"
    "v_xor_b32 v21, s34, v21\n"
    "v_xor_b32 v25, s34, v25\n"
    "v_xor_b32 v29, s34, v29\n"
    "v_xor_b32 v19, s35, v19\n"
    "v_xor_b32 v23, s35, v23\n"
    "v_xor_b32 v27, s35, v27\n"
    "v_xor_b32 v31, s35, v31\n"
    "v_or3_b32 v1, v17, v19, v21\n"
    "v_or3_b32 v1, v1, v23, v25\n"
    "v_or3_b32 v1, v1, v27, v29\n"
    "v_or_b32 v1, v1, v31\n"
    "v_cmp_ne_u32 vcc, 0, v1\n"
    "s_cmp_eq_u64 vcc, 0\n"
    "s_cbranch_scc1 .Lg_d\n"
    "s_cmp_lt_u32 s36, 32768\n"
    "s_cbranch_scc1 .Lg_l\n"
    ".Lg_d:\n"
    "v_mov_b32 v2, 155648\n"
    "v_lshl_add_u32 v2, v0, 3, v2\n"
    "ds_write2_b32 v2, v16, v18 offset0:0 offset1:1\n"
    "ds_write2_b32 v2, v20, v22 offset0:128 offset1:129\n"
    "v_add_u32 v2, 0x400, v2\n"
    "ds_write2_b32 v2, v24, v26 offset0:0 offset1:1\n"
    "ds_write2_b32 v2, v28, v30 offset0:128 offset1:129\n"
    ".Lg_b:\n"
    "s_cmp_lt_u32 s36, 2\n"
    "s_cbranch_scc0 .Lg_e\n"
    "s_barrier\n"
    "s_add_u32 s36, s36, 1\n"
    "s_branch .Lg_b\n"
    ".Lg_e:\n"
    "s_waitcnt lgkmcnt(0)\n"
    "s_barrier\n"
    "s_branch .Lph2\n"
    ".Lspare:\n"
    "s_sub_u32 s52, s33, 224\n"
    "v_and_b32 v14, 15, v0\n"
    "v_lshrrev_b32 v15, 4, v0\n"
    "s_cmp_eq_u32 s24, 14\n"
    "s_cbranch_scc0 .Lsp_0\n"
    "v_mov_b32 v1, 24260608\n"
    "s_waitcnt lgkmcnt(0)\n"
    "global_load_dwordx2 v[2:3], v1, s[10:11] sc1\n"
    "v_lshl_add_u32 v4, v0, 2, v13\n"
    "v_mov_b32 v5, 0\n"
    "ds_write_b32 v4, v5\n"
    ".Lsp_0:\n"
    "s_lshl4_add_u32 s53, s52, s24\n"
    "s_lshl_b32 s40, s53, 12\n"
    "v_lshlrev_b32 v6, 4, v14\n"
    "v_lshl_or_b32 v6, v15, 10, v6\n"
    "s_waitcnt lgkmcnt(0)\n"
    "s_add_u32 s42, s4, s40\n"
    "s_addc_u32 s43, s5, 0\n"
    "global_load_dwordx4 v[16:19], v6, s[42:43] offset:0\n"
    "global_load_dwordx4 v[20:23], v6, s[42:43] offset:256\n"
    "global_load_dwordx4 v[24:27], v6, s[42:43] offset:512\n"
    "global_load_dwordx4 v[28:31], v6, s[42:43] offset:768\n"
    "s_lshr_b32 s40, s24, 3\n"
    "s_min_u32 s41, s52, 25\n"
    "s_lshl1_add_u32 s40, s41, s40\n"
    "s_sub_u32 s41, s40, 24\n"
    "s_cmp_lt_u32 s40, 24\n"
    "s_cselect_b32 s40, s40, s41\n"
    "s_movk_i32 s54, 0xe00\n"
    "s_cselect_b32 s54, 0xc00, s54\n"
    "s_cselect_b64 s[42:43], s[6:7], s[8:9]\n"
    "s_lshl_b32 s40, s40, 7\n"
    "s_and_b32 s41, s24, 7\n"
    "s_mul_i32 s41, s41, s54\n"
    "s_lshl_b32 s41, s41, 6\n"
    "s_add_u32 s40, s40, s41\n"
    "s_add_u32 s42, s42, s40\n"
    "s_addc_u32 s43, s43, 0\n"
    "v_lshrrev_b32 v7, 3, v0\n"
    "v_and_b32 v8, 7, v0\n"
    "v_lshlrev_b32 v9, 4, v8\n"
    "v_mad_u32_u24 v9, v7, s54, v9\n"
    "s_lshl_b32 s54, s54, 3\n"
    "s_cmp_lt_u32 s52, 26\n"
    "s_cselect_b64 vcc, -1, 0\n"
    "v_cndmask_b32 v9, 0, v9, vcc\n"
    "global_load_dwordx4 v[32:35], v9, s[42:43]\n"
    "s_add_u32 s42, s42, s54\n"
    "s_addc_u32 s43, s43, 0\n"
    "global_load_dwordx4 v[36:39], v9, s[42:43]\n"
    "s_add_u32 s42, s42, s54\n"
    "s_addc_u32 s43, s43, 0\n"
    "global_load_dwordx4 v[40:43], v9, s[42:43]\n"
    "s_add_u32 s42, s42, s54\n"
    "s_addc_u32 s43, s43, 0\n"
    "global_load_dwordx4 v[44:47], v9, s[42:43]\n"
    "s_add_u32 s42, s42, s54\n"
    "s_addc_u32 s43, s43, 0\n"
    "global_load_dwordx4 v[48:51], v9, s[42:43]\n"
    "s_add_u32 s42, s42, s54\n"
    "s_addc_u32 s43, s43, 0\n"
    "global_load_dwordx4 v[52:55], v9, s[42:43]\n"
    "s_add_u32 s42, s42, s54\n"
    "s_addc_u32 s43, s43, 0\n"
    "global_load_dwordx4 v[56:59], v9, s[42:43]\n"
    "s_add_u32 s42, s42, s54\n"
    "s_addc_u32 s43, s43, 0\n"
    "global_load_dwordx4 v[60:63], v9, s[42:43]\n"
    "s_cmp_eq_u32 s24, 14\n"
    "s_cbranch_scc0 .Lsp_1\n"
    "s_waitcnt vmcnt(12)\n"
    "v_add_u32 v2, 1, v2\n"
    "v_add_u32 v3, 1, v3\n"
    "ds_write_b64 v13, v[2:3] offset:72\n"
    ".Lsp_1:\n"
    "v_and_b32 v1, 1, v14\n"
    "v_lshlrev_b32 v1, 4, v1\n"
    "v_mov_b32 v10, 0x3f80\n"
    "v_lshlrev_b32 v10, v1, v10\n"
    "v_lshrrev_b32 v1, 1, v14\n"
    "v_cmp_eq_u32 vcc, 0, v1\n"
    "s_nop 1\n"
    "v_cndmask_b32 v64, 0, v10, vcc\n"
    "v_cmp_eq_u32 vcc, 1, v1\n"
    "s_nop 1\n"
    "v_cndmask_b32 v65, 0, v10, vcc\n"
    "v_cmp_eq_u32 vcc, 2, v1\n"
    "s_nop 1\n"
    "v_cndmask_b32 v66, 0, v10, vcc\n"
    "v_cmp_eq_u32 vcc, 3, v1\n"
    "s_nop 1\n"
    "v_cndmask_b32 v67, 0, v10, vcc\n"
    "v_mov_b32 v10, 0\n"
    "v_mov_b32 v11, 0\n"
    "v_mov_b32 v12, 0x3f803f80\n"
    "s_waitcnt vmcnt(11)\n"
    "v_dot2_f32_bf16 v10, v16, v12, v10\n"
    "v_dot2_f32_bf16 v11, v16, v16, v11\n"
    "v_dot2_f32_bf16 v10, v17, v12, v10\n"
    "v_dot2_f32_bf16 v11, v17, v17, v11\n"
    "v_dot2_f32_bf16 v10, v18, v12, v10\n"
    "v_dot2_f32_bf16 v11, v18, v18, v11\n"
    "v_dot2_f32_bf16 v10, v19, v12, v10\n"
    "v_dot2_f32_bf16 v11, v19, v19, v11\n"
    "s_waitcnt vmcnt(10)\n"
    "v_dot2_f32_bf16 v10, v20, v12, v10\n"
    "v_dot2_f32_bf16 v11, v20, v20, v11\n"
    "v_dot2_f32_bf16 v10, v21, v12, v10\n"
    "v_dot2_f32_bf16 v11, v21, v21, v11\n"
    "v_dot2_f32_bf16 v10, v22, v12, v10\n"
    "v_dot2_f32_bf16 v11, v22, v22, v11\n"
    "v_dot2_f32_bf16 v10, v23, v12, v10\n"
    "v_dot2_f32_bf16 v11, v23, v23, v11\n"
    "s_waitcnt vmcnt(9)\n"
    "v_dot2_f32_bf16 v10, v24, v12, v10\n"
    "v_dot2_f32_bf16 v11, v24, v24, v11\n"
    "v_dot2_f32_bf16 v10, v25, v12, v10\n"
    "v_dot2_f32_bf16 v11, v25, v25, v11\n"
    "v_dot2_f32_bf16 v10, v26, v12, v10\n"
    "v_dot2_f32_bf16 v11, v26, v26, v11\n"
    "v_dot2_f32_bf16 v10, v27, v12, v10\n"
    "v_dot2_f32_bf16 v11, v27, v27, v11\n"
    "s_waitcnt vmcnt(8)\n"
    "v_dot2_f32_bf16 v10, v28, v12, v10\n"
    "v_dot2_f32_bf16 v11, v28, v28, v11\n"
    "v_dot2_f32_bf16 v10, v29, v12, v10\n"
    "v_dot2_f32_bf16 v11, v29, v29, v11\n"
    "v_dot2_f32_bf16 v10, v30, v12, v10\n"
    "v_dot2_f32_bf16 v11, v30, v30, v11\n"
    "v_dot2_f32_bf16 v10, v31, v12, v10\n"
    "v_dot2_f32_bf16 v11, v31, v31, v11\n"
    "s_nop 2\n"
    "v_add_f32_dpp v10, v10, v10 row_ror:8 row_mask:0xf bank_mask:0xf\n"
    "v_add_f32_dpp v11, v11, v11 row_ror:8 row_mask:0xf bank_mask:0xf\n"
    "s_nop 0\n"
    "v_add_f32_dpp v10, v10, v10 row_ror:4 row_mask:0xf bank_mask:0xf\n"
    "v_add_f32_dpp v11, v11, v11 row_ror:4 row_mask:0xf bank_mask:0xf\n"
    "s_nop 0\n"
    "v_add_f32_dpp v10, v10, v10 row_ror:2 row_mask:0xf bank_mask:0xf\n"
    "v_add_f32_dpp v11, v11, v11 row_ror:2 row_mask:0xf bank_mask:0xf\n"
    "s_nop 0\n"
    "v_add_f32_dpp v10, v10, v10 row_ror:1 row_mask:0xf bank_mask:0xf\n"
    "v_add_f32_dpp v11, v11, v11 row_ror:1 row_mask:0xf bank_mask:0xf\n"
    "s_nop 0\n"
    "v_mul_f32 v10, 0x3b000000, v10\n"
    "v_mul_f32 v11, 0x3b000000, v11\n"
    "v_fma_f32 v11, -v10, v10, v11\n"
    "v_max_f32 v11, 0, v11\n"
    "v_cvt_pk_bf16_f32 v11, v11, v11\n"
    "v_lshlrev_b32 v11, 16, v11\n"
    "v_add_f32 v11, 0x3727c5ac, v11\n"
    "v_cvt_pk_bf16_f32 v11, v11, v11\n"
    "v_lshlrev_b32 v11, 16, v11\n"
    "v_sqrt_f32 v11, v11\n"
    "v_cvt_pk_bf16_f32 v10, v10, v10\n"
    "v_lshlrev_b32 v10, 16, v10\n"
    "v_cvt_pk_bf16_f32 v11, v11, v11\n"
    "v_lshlrev_b32 v11, 16, v11\n"
    "v_rcp_f32 v11, v11\n"
    "v_xor_b32 v10, 0x80000000, v10\n"
    "s_waitcnt vmcnt(7)\n"
    "v_mfma_f32_16x16x32_bf16 v[68:71], v[64:67], v[32:35], 0\n"
    "s_waitcnt vmcnt(6)\n"
    "v_mfma_f32_16x16x32_bf16 v[68:71], v[64:67], v[36:39], v[68:71]\n"
    "s_waitcnt vmcnt(5)\n"
    "v_mfma_f32_16x16x32_bf16 v[68:71], v[64:67], v[40:43], v[68:71]\n"
    "s_waitcnt vmcnt(4)\n"
    "v_mfma_f32_16x16x32_bf16 v[68:71], v[64:67], v[44:47], v[68:71]\n"
    "s_waitcnt vmcnt(3)\n"
    "v_mfma_f32_16x16x32_bf16 v[68:71], v[64:67], v[48:51], v[68:71]\n"
    "s_waitcnt vmcnt(2)\n"
    "v_mfma_f32_16x16x32_bf16 v[68:71], v[64:67], v[52:55], v[68:71]\n"
    "s_waitcnt vmcnt(1)\n"
    "v_mfma_f32_16x16x32_bf16 v[68:71], v[64:67], v[56:59], v[68:71]\n"
    "s_waitcnt vmcnt(0)\n"
    "v_mfma_f32_16x16x32_bf16 v[68:71], v[64:67], v[60:63], v[68:71]\n"
    "s_lshl_b32 s40, s24, 8\n"
    "s_add_u32 s40, s40, 147456\n"
    "v_lshlrev_b32 v1, 5, v14\n"
    "v_lshl_or_b32 v1, v15, 4, v1\n"
    "v_add_u32 v1, s40, v1\n"
    "s_nop 2\n"
    "v_add_f32_dpp v68, v68, v68 row_ror:8 row_mask:0xf bank_mask:0xf\n"
    "v_add_f32_dpp v69, v69, v69 row_ror:8 row_mask:0xf bank_mask:0xf\n"
    "v_add_f32_dpp v70, v70, v70 row_ror:8 row_mask:0xf bank_mask:0xf\n"
    "v_add_f32_dpp v71, v71, v71 row_ror:8 row_mask:0xf bank_mask:0xf\n"
    "s_mov_b64 exec, 0x00ff00ff\n"
    "ds_write_b128 v1, v[68:71]\n"
    "s_mov_b64 exec, -1\n"
    "s_waitcnt lgkmcnt(0)\n"
    "s_barrier\n"
    "ds_read_b64 v[2:3], v13 offset:72\n"
    "s_waitcnt lgkmcnt(0)\n"
    "v_readfirstlane_b32 s34, v2\n"
    "v_readfirstlane_b32 s35, v3\n"
    "s_nop 1\n"
    "v_mov_b32 v4, v10\n"
    "v_mov_b32 v5, v2\n"
    "v_mov_b32 v6, v11\n"
    "v_mov_b32 v7, v3\n"
    "s_lshl_b32 s40, s53, 6\n"
    "s_add_u32 s40, s40, 23068672\n"
    "v_lshl_add_u32 v1, v15, 4, s40\n"
    "s_mov_b32 exec_lo, 0x00010001\n"
    "s_mov_b32 exec_hi, 0x00010001\n"
    "global_store_dwordx4 v1, v[4:7], s[10:11] sc1\n"
    "s_mov_b64 exec, -1\n"
    "s_cmp_eq_u32 s24, 8\n"
    "s_cselect_b32 s40, s52, 99\n"
    "s_cmp_lt_u32 s40, 26\n"
    "s_cbranch_scc0 .Lsp_2\n"
    "v_lshrrev_b32 v1, 5, v0\n"
    "v_lshlrev_b32 v1, 11, v1\n"
    "v_and_b32 v9, 31, v0\n"
    "v_lshl_or_b32 v1, v9, 3, v1\n"
    "v_add_u32 v1, 147456, v1\n"
    "ds_read_b64 v[16:17], v1 offset:0\n"
    "ds_read_b64 v[18:19], v1 offset:256\n"
    "ds_read_b64 v[20:21], v1 offset:512\n"
    "ds_read_b64 v[22:23], v1 offset:768\n"
    "ds_read_b64 v[24:25], v1 offset:1024\n"
    "ds_read_b64 v[26:27], v1 offset:1280\n"
    "ds_read_b64 v[28:29], v1 offset:1536\n"
    "ds_read_b64 v[30:31], v1 offset:1792\n"
    "s_waitcnt lgkmcnt(0)\n"
    "v_pk_add_f32 v[16:17], v[16:17], v[18:19]\n"
    "v_pk_add_f32 v[16:17], v[16:17], v[20:21]\n"
    "v_pk_add_f32 v[16:17], v[16:17], v[22:23]\n"
    "v_pk_add_f32 v[16:17], v[16:17], v[24:25]\n"
    "v_pk_add_f32 v[16:17], v[16:17], v[26:27]\n"
    "v_pk_add_f32 v[16:17], v[16:17], v[28:29]\n"
    "v_pk_add_f32 v[16:17], v[16:17], v[30:31]\n"
    "v_mov_b32 v18, v17\n"
    "v_mov_b32 v17, v2\n"
    "v_mov_b32 v19, v3\n"
    "s_lshl_b32 s40, s52, 10\n"
    "s_add_u32 s40, s40, 23101440\n"
    "v_lshl_add_u32 v1, v0, 4, s40\n"
    "global_store_dwordx4 v1, v[16:19], s[10:11] sc1\n"
    ".Lsp_2:\n"
    "s_lshl_b32 s40, s24, 5\n"
    "s_add_u32 s40, s40, s52\n"
    "s_cmp_lt_u32 s40, 176\n"
    "s_cbranch_scc0 .Lsp_3\n"
    "s_lshr_b32 s54, s40, 3\n"
    "s_and_b32 s55, s40, 7\n"
    "s_sub_u32 s41, s54, 8\n"
    "s_cmp_lt_u32 s54, 8\n"
    "s_cselect_b32 s41, s54, s41\n"
    "s_cselect_b64 s[42:43], s[12:13], s[14:15]\n"
    "s_lshl_b32 s41, s41, 9\n"
    "s_add_u32 s41, s41, s55\n"
    "s_lshl_b32 s41, s41, 7\n"
    "s_add_u32 s42, s42, s41\n"
    "s_addc_u32 s43, s43, 0\n"
    "v_lshrrev_b32 v20, 3, v0\n"
    "v_and_b32 v21, 2, v20\n"
    "v_xor_b32 v21, v21, v8\n"
    "v_lshlrev_b32 v21, 4, v21\n"
    "v_lshl_or_b32 v20, v20, 10, v21\n"
    "v_xor_b32 v21, 64, v20\n"
    "s_lshl_b32 s41, s24, 13\n"
    "s_add_u32 s41, s41, 98304\n"
    "s_mov_b32 m0, s41\n"
    "s_nop 0\n"
    "global_load_lds_dwordx4 v20, s[42:43] nt\n"
    "s_add_u32 m0, m0, 0x400\n"
    "s_add_u32 s42, s42, 0x2000\n"
    "s_addc_u32 s43, s43, 0\n"
    "global_load_lds_dwordx4 v21, s[42:43] nt\n"
    "s_add_u32 m0, m0, 0x400\n"
    "s_add_u32 s42, s42, 0x2000\n"
    "s_addc_u32 s43, s43, 0\n"
    "global_load_lds_dwordx4 v20, s[42:43] nt\n"
    "s_add_u32 m0, m0, 0x400\n"
    "s_add_u32 s42, s42, 0x2000\n"
    "s_addc_u32 s43, s43, 0\n"
    "global_load_lds_dwordx4 v21, s[42:43] nt\n"
    "s_add_u32 m0, m0, 0x400\n"
    "s_add_u32 s42, s42, 0x2000\n"
    "s_addc_u32 s43, s43, 0\n"
    "global_load_lds_dwordx4 v20, s[42:43] nt\n"
    "s_add_u32 m0, m0, 0x400\n"
    "s_add_u32 s42, s42, 0x2000\n"
    "s_addc_u32 s43, s43, 0\n"
    "global_load_lds_dwordx4 v21, s[42:43] nt\n"
    "s_add_u32 m0, m0, 0x400\n"
    "s_add_u32 s42, s42, 0x2000\n"
    "s_addc_u32 s43, s43, 0\n"
    "global_load_lds_dwordx4 v20, s[42:43] nt\n"
    "s_add_u32 m0, m0, 0x400\n"
    "s_add_u32 s42, s42, 0x2000\n"
    "s_addc_u32 s43, s43, 0\n"
    "global_load_lds_dwordx4 v21, s[42:43] nt\n"
    "s_add_u32 m0, m0, 0x400\n"
    "s_add_u32 s42, s42, 0x2000\n"
    "s_addc_u32 s43, s43, 0\n"
    "v_and_b32 v24, 0x30, v0\n"
    "v_lshlrev_b32 v24, 6, v24\n"
    "v_and_b32 v1, 12, v0\n"
    "v_lshl_or_b32 v24, v1, 5, v24\n"
    "v_and_b32 v1, 24, v0\n"
    "v_lshl_or_b32 v24, v1, 2, v24\n"
    "v_and_b32 v1, 3, v0\n"
    "v_lshl_or_b32 v24, v1, 3, v24\n"
    "v_add_u32 v24, s41, v24\n"
    "v_xor_b32 v25, 32, v24\n"
    "v_xor_b32 v26, 64, v24\n"
    "v_xor_b32 v27, 96, v24\n"
    "v_lshlrev_b32 v1, 4, v14\n"
    "v_lshl_or_b32 v1, v15, 9, v1\n"
    "s_mul_i32 s41, s55, 44\n"
    "s_add_u32 s41, s41, s54\n"
    "s_lshl_b32 s41, s41, 12\n"
    "s_add_u32 s41, s41, 16777216\n"
    "s_add_u32 s42, s10, s41\n"
    "s_addc_u32 s43, s11, 0\n"
    "s_add_u32 s44, s42, 90112\n"
    "s_addc_u32 s45, s43, 0\n"
    "s_waitcnt vmcnt(0)\n"
    "ds_read_b64_tr_b16 v[32:33], v24 offset:0\n"
    "ds_read_b64_tr_b16 v[34:35], v24 offset:512\n"
    "ds_read_b64_tr_b16 v[36:37], v25 offset:0\n"
    "ds_read_b64_tr_b16 v[38:39], v25 offset:512\n"
    "ds_read_b64_tr_b16 v[40:41], v26 offset:0\n"
    "ds_read_b64_tr_b16 v[42:43], v26 offset:512\n"
    "ds_read_b64_tr_b16 v[44:45], v27 offset:0\n"
    "ds_read_b64_tr_b16 v[46:47], v27 offset:512\n"
    "s_waitcnt lgkmcnt(0)\n"
    "global_store_dwordx4 v1, v[32:35], s[42:43] offset:0 sc1\n"
    "global_store_dwordx4 v1, v[36:39], s[42:43] offset:256 sc1\n"
    "global_store_dwordx4 v1, v[40:43], s[44:45] offset:0 sc1\n"
    "global_store_dwordx4 v1, v[44:47], s[44:45] offset:256 sc1\n"
    "ds_read_b64_tr_b16 v[48:49], v24 offset:4096\n"
    "ds_read_b64_tr_b16 v[50:51], v24 offset:4608\n"
    "ds_read_b64_tr_b16 v[52:53], v25 offset:4096\n"
    "ds_read_b64_tr_b16 v[54:55], v25 offset:4608\n"
    "ds_read_b64_tr_b16 v[56:57], v26 offset:4096\n"
    "ds_read_b64_tr_b16 v[58:59], v26 offset:4608\n"
    "ds_read_b64_tr_b16 v[60:61], v27 offset:4096\n"
    "ds_read_b64_tr_b16 v[62:63], v27 offset:4608\n"
    "s_waitcnt lgkmcnt(0)\n"
    "global_store_dwordx4 v1, v[48:51], s[42:43] offset:2048 sc1\n"
    "global_store_dwordx4 v1, v[52:55], s[42:43] offset:2304 sc1\n"
    "global_store_dwordx4 v1, v[56:59], s[44:45] offset:2048 sc1\n"
    "global_store_dwordx4 v1, v[60:63], s[44:45] offset:2304 sc1\n"
    "s_lshl_b32 s41, s55, 5\n"
    "s_add_u32 s41, s41, s54\n"
    "s_lshl_b32 s41, s41, 7\n"
    "s_add_u32 s41, s41, 24227840\n"
    "v_mov_b32 v2, s34\n"
    "v_mov_b32 v3, s35\n"
    "v_lshl_add_u32 v4, v0, 3, s41\n"
    "s_waitcnt vmcnt(0)\n"
    "s_mov_b64 exec, 0xffff\n"
    "global_store_dwordx2 v4, v[2:3], s[10:11] sc1\n"
    "s_mov_b64 exec, -1\n"
    ".Lsp_3:\n"
    "s_cmp_lt_u32 s24, 4\n"
    "s_cbranch_scc0 .Lsp_4\n"
    "s_waitcnt lgkmcnt(0)\n"
    "s_mov_b64 exec, 1\n"
    "v_mov_b32 v1, 1\n"
    "ds_add_u32 v13, v1 offset:48\n"
    "s_mov_b64 exec, -1\n"
    ".Lsp_4:\n"
    "s_cmp_lt_u32 s24, 8\n"
    "s_cbranch_scc1 .Lattn\n"
    ".Lph2:\n"
    "s_setprio 3\n"
    "v_mov_b32 v13, 157696\n"
    "s_waitcnt vmcnt(0)\n"
    "ds_read_b64 v[2:3], v13 offset:72\n"
    "s_waitcnt lgkmcnt(0)\n"
    "v_readfirstlane_b32 s34, v2\n"
    "v_readfirstlane_b32 s35, v3\n"
    "s_nop 1\n"
    "s_lshl2_add_u32 s52, s25, s27\n"
    "s_lshr_b32 s53, s26, 3\n"
    "s_lshl2_add_u32 s53, s25, s53\n"
    "s_and_b32 s54, s26, 7\n"
    "s_cmp_lt_u32 s24, 12\n"
    "s_cbranch_scc1 .Lgw\n"
    "s_cmp_eq_u32 s24, 13\n"
    "s_cbranch_scc1 .Lwld\n"
    "s_cmp_eq_u32 s24, 14\n"
    "s_cbranch_scc1 .Lsync\n"
    "s_cmp_eq_u32 s24, 15\n"
    "s_cbranch_scc1 .Lqz\n"
    "s_and_b32 s40, s26, 3\n"
    "s_lshl2_add_u32 s40, s25, s40\n"
    "s_lshl_b32 s40, s40, 17\n"
    "s_add_u32 s40, s40, 2097152\n"
    "s_add_u32 s46, s10, s40\n"
    "s_addc_u32 s47, s11, 0\n"
    "s_and_b32 s38, s26, 28\n"
    "v_add_u32 v2, s38, v0\n"
    "s_lshl_b32 s38, s38, 12\n"
    "s_add_u32 s42, s46, s38\n"
    "s_addc_u32 s43, s47, 0\n"
    "v_lshrrev_b32 v1, 5, v0\n"
    "v_lshlrev_b32 v1, 8, v1\n"
    "v_and_b32 v2, 31, v2\n"
    "v_add_u32 v1, v1, v2\n"
    "s_lshl_b32 s40, s25, 12\n"
    "s_add_u32 s40, s40, 23625728\n"
    "v_lshl_add_u32 v4, v1, 7, s40\n"
    "v_lshlrev_b32 v5, 4, v0\n"
    "s_sleep 16\n"
    ".Lw_5:\n"
    "ds_read_b32 v1, v13 offset:48\n"
    "s_waitcnt lgkmcnt(0)\n"
    "v_readfirstlane_b32 s60, v1\n"
    "s_cmp_ge_u32 s60, 4\n"
    "s_cbranch_scc1 .Ld_6\n"
    "s_sleep 1\n"
    "s_add_u32 s63, s63, 1\n"
    "s_bitcmp1_b32 s63, 19\n"
    "s_cbranch_scc0 .Lw_5\n"
    "s_sub_u32 s63, s63, 1\n"
    ".Ld_6:\n"
    "s_mov_b32 exec_lo, 0xf\n"
    "s_mov_b32 exec_hi, 0xf\n"
    "s_mov_b32 s61, 0\n"
    ".Lp_7:\n"
    "global_load_dwordx2 v[2:3], v4, s[10:11] sc1\n"
    "s_waitcnt vmcnt(0)\n"
    "v_cmp_ne_u64 vcc, s[34:35], v[2:3]\n"
    "s_add_u32 s61, s61, 1\n"
    "s_and_b64 exec, exec, vcc\n"
    "s_cbranch_scc0 .Ld_8\n"
    "s_sleep 2\n"
    "s_cmp_lt_u32 s61, 32768\n"
    "s_cbranch_scc1 .Lp_7\n"
    ".Ld_8:\n"
    "s_mov_b64 exec, -1\n"
    "s_mov_b32 m0, 0\n"
    "s_add_u32 s42, s46, s38\n"
    "s_addc_u32 s43, s47, 0\n"
    "s_mov_b64 exec, -1\n"
    "global_load_lds_dwordx4 v5, s[42:43] offset:0 sc1\n"
    "global_load_lds_dwordx4 v5, s[42:43] offset:1024 sc1\n"
    "global_load_lds_dwordx4 v5, s[42:43] offset:2048 sc1\n"
    "global_load_lds_dwordx4 v5, s[42:43] offset:3072 sc1\n"
    "s_add_u32 s38, s38, 0x1000\n"
    "s_mov_b64 exec, 1\n"
    "s_mov_b32 m0, 4096\n"
    "s_add_u32 s42, s46, s38\n"
    "s_addc_u32 s43, s47, 0\n"
    "s_mov_b64 exec, -1\n"
    "global_load_lds_dwordx4 v5, s[42:43] offset:0 sc1\n"
    "global_load_lds_dwordx4 v5, s[42:43] offset:1024 sc1\n"
    "global_load_lds_dwordx4 v5, s[42:43] offset:2048 sc1\n"
    "global_load_lds_dwordx4 v5, s[42:43] offset:3072 sc1\n"
    "s_add_u32 s38, s38, 0x1000\n"
    "s_mov_b64 exec, 1\n"
    "s_mov_b32 m0, 8192\n"
    "s_add_u32 s42, s46, s38\n"
    "s_addc_u32 s43, s47, 0\n"
    "s_mov_b64 exec, -1\n"
    "global_load_lds_dwordx4 v5, s[42:43] offset:0 sc1\n"
    "global_load_lds_dwordx4 v5, s[42:43] offset:1024 sc1\n"
    "global_load_lds_dwordx4 v5, s[42:43] offset:2048 sc1\n"
    "global_load_lds_dwordx4 v5, s[42:43] offset:3072 sc1\n"
    "s_add_u32 s38, s38, 0x1000\n"
    "s_mov_b64 exec, 1\n"
    "s_mov_b32 m0, 12288\n"
    "s_add_u32 s42, s46, s38\n"
    "s_addc_u32 s43, s47, 0\n"
    "s_mov_b64 exec, -1\n"
    "global_load_lds_dwordx4 v5, s[42:43] offset:0 sc1\n"
    "global_load_lds_dwordx4 v5, s[42:43] offset:1024 sc1\n"
    "global_load_lds_dwordx4 v5, s[42:43] offset:2048 sc1\n"
    "global_load_lds_dwordx4 v5, s[42:43] offset:3072 sc1\n"
    "s_add_u32 s38, s38, 0x1000\n"
    "s_and_b32 s38, s38, 0x1ffff\n"
    "s_mov_b64 exec, 1\n"
    "v_mov_b32 v1, 2\n"
    "s_waitcnt vmcnt(12)\n"
    "ds_write_b32 v13, v1 offset:0\n"
    "v_mov_b32 v1, 6\n"
    "s_waitcnt vmcnt(4)\n"
    "ds_write_b32 v13, v1 offset:0\n"
    "v_mov_b32 v1, 8\n"
    "s_waitcnt vmcnt(0)\n"
    "ds_write_b32 v13, v1 offset:0\n"
    "s_mov_b32 exec_lo, 0xff0\n"
    "s_mov_b32 exec_hi, 0xff0\n"
    "s_mov_b32 s61, 0\n"
    ".Lp_9:\n"
    "global_load_dwordx2 v[2:3], v4, s[10:11] sc1\n"
    "s_waitcnt vmcnt(0)\n"
    "v_cmp_ne_u64 vcc, s[34:35], v[2:3]\n"
    "s_add_u32 s61, s61, 1\n"
    "s_and_b64 exec, exec, vcc\n"
    "s_cbranch_scc0 .Ld_10\n"
    "s_sleep 2\n"
    "s_cmp_lt_u32 s61, 32768\n"
    "s_cbranch_scc1 .Lp_9\n"
    ".Ld_10:\n"
    "s_mov_b64 exec, -1\n"
    "s_mov_b32 m0, 16384\n"
    "s_add_u32 s42, s46, s38\n"
    "s_addc_u32 s43, s47, 0\n"
    "s_mov_b64 exec, -1\n"
    "global_load_lds_dwordx4 v5, s[42:43] offset:0 sc1\n"
    "global_load_lds_dwordx4 v5, s[42:43] offset:1024 sc1\n"
    "global_load_lds_dwordx4 v5, s[42:43] offset:2048 sc1\n"
    "global_load_lds_dwordx4 v5, s[42:43] offset:3072 sc1\n"
    "s_add_u32 s38, s38, 0x1000\n"
    "s_mov_b64 exec, 1\n"
    "s_mov_b32 m0, 20480\n"
    "s_add_u32 s42, s46, s38\n"
    "s_addc_u32 s43, s47, 0\n"
    "s_mov_b64 exec, -1\n"
    "global_load_lds_dwordx4 v5, s[42:43] offset:0 sc1\n"
    "global_load_lds_dwordx4 v5, s[42:43] offset:1024 sc1\n"
    "global_load_lds_dwordx4 v5, s[42:43] offset:2048 sc1\n"
    "global_load_lds_dwordx4 v5, s[42:43] offset:3072 sc1\n"
    "s_add_u32 s38, s38, 0x1000\n"
    "s_mov_b64 exec, 1\n"
    "s_mov_b32 m0, 24576\n"
    "s_add_u32 s42, s46, s38\n"
    "s_addc_u32 s43, s47, 0\n"
    "s_mov_b64 exec, -1\n"
    "global_load_lds_dwordx4 v5, s[42:43] offset:0 sc1\n"
    "global_load_lds_dwordx4 v5, s[42:43] offset:1024 sc1\n"
    "global_load_lds_dwordx4 v5, s[42:43] offset:2048 sc1\n"
    "global_load_lds_dwordx4 v5, s[42:43] offset:3072 sc1\n"
    "s_add_u32 s38, s38, 0x1000\n"
    "s_mov_b64 exec, 1\n"
    "s_mov_b32 m0, 28672\n"
    "s_add_u32 s42, s46, s38\n"
    "s_addc_u32 s43, s47, 0\n"
    "s_mov_b64 exec, -1\n"
    "global_load_lds_dwordx4 v5, s[42:43] offset:0 sc1\n"
    "global_load_lds_dwordx4 v5, s[42:43] offset:1024 sc1\n"
    "global_load_lds_dwordx4 v5, s[42:43] offset:2048 sc1\n"
    "global_load_lds_dwordx4 v5, s[42:43] offset:3072 sc1\n"
    "s_add_u32 s38, s38, 0x1000\n"
    "s_and_b32 s38, s38, 0x1ffff\n"
    "s_mov_b64 exec, 1\n"
    "s_mov_b32 m0, 32768\n"
    "s_add_u32 s42, s46, s38\n"
    "s_addc_u32 s43, s47, 0\n"
    "s_mov_b64 exec, -1\n"
    "global_load_lds_dwordx4 v5, s[42:43] offset:0 sc1\n"
    "global_load_lds_dwordx4 v5, s[42:43] offset:1024 sc1\n"
    "global_load_lds_dwordx4 v5, s[42:43] offset:2048 sc1\n"
    "global_load_lds_dwordx4 v5, s[42:43] offset:3072 sc1\n"
    "s_add_u32 s38, s38, 0x1000\n"
    "s_mov_b64 exec, 1\n"
    "v_mov_b32 v1, 10\n"
    "s_waitcnt vmcnt(16)\n"
    "ds_write_b32 v13, v1 offset:0\n"
    "s_mov_b32 m0, 36864\n"
    "s_add_u32 s42, s46, s38\n"
    "s_addc_u32 s43, s47, 0\n"
    "s_mov_b64 exec, -1\n"
    "global_load_lds_dwordx4 v5, s[42:43] offset:0 sc1\n"
    "global_load_lds_dwordx4 v5, s[42:43] offset:1024 sc1\n"
    "global_load_lds_dwordx4 v5, s[42:43] offset:2048 sc1\n"
    "global_load_lds_dwordx4 v5, s[42:43] offset:3072 sc1\n"
    "s_add_u32 s38, s38, 0x1000\n"
    "s_mov_b64 exec, 1\n"
    "v_mov_b32 v1, 12\n"
    "s_waitcnt vmcnt(16)\n"
    "ds_write_b32 v13, v1 offset:0\n"
    "s_mov_b32 m0, 40960\n"
    "s_add_u32 s42, s46, s38\n"
    "s_addc_u32 s43, s47, 0\n"
    "s_mov_b64 exec, -1\n"
    "global_load_lds_dwordx4 v5, s[42:43] offset:0 sc1\n"
    "global_load_lds_dwordx4 v5, s[42:43] offset:1024 sc1\n"
    "global_load_lds_dwordx4 v5, s[42:43] offset:2048 sc1\n"
    "global_load_lds_dwordx4 v5, s[42:43] offset:3072 sc1\n"
    "s_add_u32 s38, s38, 0x1000\n"
    "s_mov_b64 exec, 1\n"
    "v_mov_b32 v1, 14\n"
    "s_waitcnt vmcnt(16)\n"
    "ds_write_b32 v13, v1 offset:0\n"
    "s_mov_b32 m0, 45056\n"
    "s_add_u32 s42, s46, s38\n"
    "s_addc_u32 s43, s47, 0\n"
    "s_mov_b64 exec, -1\n"
    "global_load_lds_dwordx4 v5, s[42:43] offset:0 sc1\n"
    "global_load_lds_dwordx4 v5, s[42:43] offset:1024 sc1\n"
    "global_load_lds_dwordx4 v5, s[42:43] offset:2048 sc1\n"
    "global_load_lds_dwordx4 v5, s[42:43] offset:3072 sc1\n"
    "s_add_u32 s38, s38, 0x1000\n"
    "s_and_b32 s38, s38, 0x1ffff\n"
    "s_mov_b64 exec, 1\n"
    "v_mov_b32 v1, 16\n"
    "s_waitcnt vmcnt(16)\n"
    "ds_write_b32 v13, v1 offset:0\n"
    "v_mov_b32 v1, 20\n"
    "s_waitcnt vmcnt(8)\n"
    "ds_write_b32 v13, v1 offset:0\n"
    "v_mov_b32 v1, 24\n"
    "s_waitcnt vmcnt(0)\n"
    "ds_write_b32 v13, v1 offset:0\n"
    "s_mov_b32 exec_lo, 0xfffff000\n"
    "s_mov_b32 exec_hi, 0xfffff000\n"
    "s_mov_b32 s61, 0\n"
    ".Lp_11:\n"
    "global_load_dwordx2 v[2:3], v4, s[10:11] sc1\n"
    "s_waitcnt vmcnt(0)\n"
    "v_cmp_ne_u64 vcc, s[34:35], v[2:3]\n"
    "s_add_u32 s61, s61, 1\n"
    "s_and_b64 exec, exec, vcc\n"
    "s_cbranch_scc0 .Ld_12\n"
    "s_sleep 2\n"
    "s_cmp_lt_u32 s61, 32768\n"
    "s_cbranch_scc1 .Lp_11\n"
    ".Ld_12:\n"
    "s_mov_b64 exec, -1\n"
    "s_mov_b32 m0, 49152\n"
    "s_add_u32 s42, s46, s38\n"
    "s_addc_u32 s43, s47, 0\n"
    "s_mov_b64 exec, -1\n"
    "global_load_lds_dwordx4 v5, s[42:43] offset:0 sc1\n"
    "global_load_lds_dwordx4 v5, s[42:43] offset:1024 sc1\n"
    "global_load_lds_dwordx4 v5, s[42:43] offset:2048 sc1\n"
    "global_load_lds_dwordx4 v5, s[42:43] offset:3072 sc1\n"
    "s_add_u32 s38, s38, 0x1000\n"
    "s_mov_b64 exec, 1\n"
    "s_mov_b32 m0, 53248\n"
    "s_add_u32 s42, s46, s38\n"
    "s_addc_u32 s43, s47, 0\n"
    "s_mov_b64 exec, -1\n"
    "global_load_lds_dwordx4 v5, s[42:43] offset:0 sc1\n"
    "global_load_lds_dwordx4 v5, s[42:43] offset:1024 sc1\n"
    "global_load_lds_dwordx4 v5, s[42:43] offset:2048 sc1\n"
    "global_load_lds_dwordx4 v5, s[42:43] offset:3072 sc1\n"
    "s_add_u32 s38, s38, 0x1000\n"
    "s_mov_b64 exec, 1\n"
    "s_mov_b32 m0, 57344\n"
    "s_add_u32 s42, s46, s38\n"
    "s_addc_u32 s43, s47, 0\n"
    "s_mov_b64 exec, -1\n"
    "global_load_lds_dwordx4 v5, s[42:43] offset:0 sc1\n"
    "global_load_lds_dwordx4 v5, s[42:43] offset:1024 sc1\n"
    "global_load_lds_dwordx4 v5, s[42:43] offset:2048 sc1\n"
    "global_load_lds_dwordx4 v5, s[42:43] offset:3072 sc1\n"
    "s_add_u32 s38, s38, 0x1000\n"
    "s_mov_b64 exec, 1\n"
    "s_mov_b32 m0, 61440\n"
    "s_add_u32 s42, s46, s38\n"
    "s_addc_u32 s43, s47, 0\n"
    "s_mov_b64 exec, -1\n"
    "global_load_lds_dwordx4 v5, s[42:43] offset:0 sc1\n"
    "global_load_lds_dwordx4 v5, s[42:43] offset:1024 sc1\n"
    "global_load_lds_dwordx4 v5, s[42:43] offset:2048 sc1\n"
    "global_load_lds_dwordx4 v5, s[42:43] offset:3072 sc1\n"
    "s_add_u32 s38, s38, 0x1000\n"
    "s_and_b32 s38, s38, 0x1ffff\n"
    "s_mov_b64 exec, 1\n"
    "s_mov_b64 exec, -1\n"
    "s_cmp_lt_u32 s26, 28\n"
    "s_cbranch_scc0 .Lkv_z\n"
    ".Lw_13:\n"
    "ds_read_b32 v1, v13 offset:64\n"
    "s_waitcnt lgkmcnt(0)\n"
    "v_readfirstlane_b32 s60, v1\n"
    "s_cmp_ge_u32 s60, 4\n"
    "s_cbranch_scc1 .Ld_14\n"
    "s_sleep 1\n"
    "s_add_u32 s63, s63, 1\n"
    "s_bitcmp1_b32 s63, 19\n"
    "s_cbranch_scc0 .Lw_13\n"
    "s_sub_u32 s63, s63, 1\n"
    ".Ld_14:\n"
    ".Lw_15:\n"
    "ds_read_b32 v1, v13 offset:108\n"
    "s_waitcnt lgkmcnt(0)\n"
    "v_readfirstlane_b32 s60, v1\n"
    "s_cmp_ge_u32 s60, 1\n"
    "s_cbranch_scc1 .Ld_16\n"
    "s_sleep 1\n"
    "s_add_u32 s63, s63, 1\n"
    "s_bitcmp1_b32 s63, 19\n"
    "s_cbranch_scc0 .Lw_15\n"
    "s_sub_u32 s63, s63, 1\n"
    ".Ld_16:\n"
    ".Lkv_z:\n"
    "s_mov_b32 m0, 65536\n"
    "s_add_u32 s42, s46, s38\n"
    "s_addc_u32 s43, s47, 0\n"
    "s_mov_b64 exec, -1\n"
    "global_load_lds_dwordx4 v5, s[42:43] offset:0 sc1\n"
    "global_load_lds_dwordx4 v5, s[42:43] offset:1024 sc1\n"
    "global_load_lds_dwordx4 v5, s[42:43] offset:2048 sc1\n"
    "global_load_lds_dwordx4 v5, s[42:43] offset:3072 sc1\n"
    "s_add_u32 s38, s38, 0x1000\n"
    "s_mov_b64 exec, 1\n"
    "v_mov_b32 v1, 26\n"
    "s_waitcnt vmcnt(16)\n"
    "ds_write_b32 v13, v1 offset:0\n"
    "s_mov_b32 m0, 69632\n"
    "s_add_u32 s42, s46, s38\n"
    "s_addc_u32 s43, s47, 0\n"
    "s_mov_b64 exec, -1\n"
    "global_load_lds_dwordx4 v5, s[42:43] offset:0 sc1\n"
    "global_load_lds_dwordx4 v5, s[42:43] offset:1024 sc1\n"
    "global_load_lds_dwordx4 v5, s[42:43] offset:2048 sc1\n"
    "global_load_lds_dwordx4 v5, s[42:43] offset:3072 sc1\n"
    "s_add_u32 s38, s38, 0x1000\n"
    "s_mov_b64 exec, 1\n"
    "v_mov_b32 v1, 28\n"
    "s_waitcnt vmcnt(16)\n"
    "ds_write_b32 v13, v1 offset:0\n"
    "s_mov_b32 m0, 73728\n"
    "s_add_u32 s42, s46, s38\n"
    "s_addc_u32 s43, s47, 0\n"
    "s_mov_b64 exec, -1\n"
    "global_load_lds_dwordx4 v5, s[42:43] offset:0 sc1\n"
    "global_load_lds_dwordx4 v5, s[42:43] offset:1024 sc1\n"
    "global_load_lds_dwordx4 v5, s[42:43] offset:2048 sc1\n"
    "global_load_lds_dwordx4 v5, s[42:43] offset:3072 sc1\n"
    "s_add_u32 s38, s38, 0x1000\n"
    "s_mov_b64 exec, 1\n"
    "v_mov_b32 v1, 30\n"
    "s_waitcnt vmcnt(16)\n"
    "ds_write_b32 v13, v1 offset:0\n"
    "s_mov_b32 m0, 77824\n"
    "s_add_u32 s42, s46, s38\n"
    "s_addc_u32 s43, s47, 0\n"
    "s_mov_b64 exec, -1\n"
    "global_load_lds_dwordx4 v5, s[42:43] offset:0 sc1\n"
    "global_load_lds_dwordx4 v5, s[42:43] offset:1024 sc1\n"
    "global_load_lds_dwordx4 v5, s[42:43] offset:2048 sc1\n"
    "global_load_lds_dwordx4 v5, s[42:43] offset:3072 sc1\n"
    "s_add_u32 s38, s38, 0x1000\n"
    "s_and_b32 s38, s38, 0x1ffff\n"
    "s_mov_b64 exec, 1\n"
    "v_mov_b32 v1, 32\n"
    "s_waitcnt vmcnt(16)\n"
    "ds_write_b32 v13, v1 offset:0\n"
    "s_mov_b32 m0, 81920\n"
    "s_add_u32 s42, s46, s38\n"
    "s_addc_u32 s43, s47, 0\n"
    "s_mov_b64 exec, -1\n"
    "global_load_lds_dwordx4 v5, s[42:43] offset:0 sc1\n"
    "global_load_lds_dwordx4 v5, s[42:43] offset:1024 sc1\n"
    "global_load_lds_dwordx4 v5, s[42:43] offset:2048 sc1\n"
    "global_load_lds_dwordx4 v5, s[42:43] offset:3072 sc1\n"
    "s_add_u32 s38, s38, 0x1000\n"
    "s_mov_b64 exec, 1\n"
    "v_mov_b32 v1, 34\n"
    "s_waitcnt vmcnt(16)\n"
    "ds_write_b32 v13, v1 offset:0\n"
    "s_mov_b32 m0, 86016\n"
    "s_add_u32 s42, s46, s38\n"
    "s_addc_u32 s43, s47, 0\n"
    "s_mov_b64 exec, -1\n"
    "global_load_lds_dwordx4 v5, s[42:43] offset:0 sc1\n"
    "global_load_lds_dwordx4 v5, s[42:43] offset:1024 sc1\n"
    "global_load_lds_dwordx4 v5, s[42:43] offset:2048 sc1\n"
    "global_load_lds_dwordx4 v5, s[42:43] offset:3072 sc1\n"
    "s_add_u32 s38, s38, 0x1000\n"
    "s_mov_b64 exec, 1\n"
    "v_mov_b32 v1, 36\n"
    "s_waitcnt vmcnt(16)\n"
    "ds_write_b32 v13, v1 offset:0\n"
    "s_mov_b32 m0, 90112\n"
    "s_add_u32 s42, s46, s38\n"
    "s_addc_u32 s43, s47, 0\n"
    "s_mov_b64 exec, -1\n"
    "global_load_lds_dwordx4 v5, s[42:43] offset:0 sc1\n"
    "global_load_lds_dwordx4 v5, s[42:43] offset:1024 sc1\n"
    "global_load_lds_dwordx4 v5, s[42:43] offset:2048 sc1\n"
    "global_load_lds_dwordx4 v5, s[42:43] offset:3072 sc1\n"
    "s_add_u32 s38, s38, 0x1000\n"
    "s_mov_b64 exec, 1\n"
    "v_mov_b32 v1, 38\n"
    "s_waitcnt vmcnt(16)\n"
    "ds_write_b32 v13, v1 offset:0\n"
    "s_mov_b32 m0, 94208\n"
    "s_add_u32 s42, s46, s38\n"
    "s_addc_u32 s43, s47, 0\n"
    "s_mov_b64 exec, -1\n"
    "global_load_lds_dwordx4 v5, s[42:43] offset:0 sc1\n"
    "global_load_lds_dwordx4 v5, s[42:43] offset:1024 sc1\n"
    "global_load_lds_dwordx4 v5, s[42:43] offset:2048 sc1\n"
    "global_load_lds_dwordx4 v5, s[42:43] offset:3072 sc1\n"
    "s_add_u32 s38, s38, 0x1000\n"
    "s_and_b32 s38, s38, 0x1ffff\n"
    "s_mov_b64 exec, 1\n"
    "v_mov_b32 v1, 40\n"
    "s_waitcnt vmcnt(16)\n"
    "ds_write_b32 v13, v1 offset:0\n"
    "s_mov_b32 m0, 98304\n"
    "s_add_u32 s42, s46, s38\n"
    "s_addc_u32 s43, s47, 0\n"
    "s_mov_b64 exec, -1\n"
    "global_load_lds_dwordx4 v5, s[42:43] offset:0 sc1\n"
    "global_load_lds_dwordx4 v5, s[42:43] offset:1024 sc1\n"
    "global_load_lds_dwordx4 v5, s[42:43] offset:2048 sc1\n"
    "global_load_lds_dwordx4 v5, s[42:43] offset:3072 sc1\n"
    "s_add_u32 s38, s38, 0x1000\n"
    "s_mov_b64 exec, 1\n"
    "v_mov_b32 v1, 42\n"
    "s_waitcnt vmcnt(16)\n"
    "ds_write_b32 v13, v1 offset:0\n"
    "s_mov_b32 m0, 102400\n"
    "s_add_u32 s42, s46, s38\n"
    "s_addc_u32 s43, s47, 0\n"
    "s_mov_b64 exec, -1\n"
    "global_load_lds_dwordx4 v5, s[42:43] offset:0 sc1\n"
    "global_load_lds_dwordx4 v5, s[42:43] offset:1024 sc1\n"
    "global_load_lds_dwordx4 v5, s[42:43] offset:2048 sc1\n"
    "global_load_lds_dwordx4 v5, s[42:43] offset:3072 sc1\n"
    "s_add_u32 s38, s38, 0x1000\n"
    "s_mov_b64 exec, 1\n"
    "v_mov_b32 v1, 44\n"
    "s_waitcnt vmcnt(16)\n"
    "ds_write_b32 v13, v1 offset:0\n"
    "s_mov_b32 m0, 106496\n"
    "s_add_u32 s42, s46, s38\n"
    "s_addc_u32 s43, s47, 0\n"
    "s_mov_b64 exec, -1\n"
    "global_load_lds_dwordx4 v5, s[42:43] offset:0 sc1\n"
    "global_load_lds_dwordx4 v5, s[42:43] offset:1024 sc1\n"
    "global_load_lds_dwordx4 v5, s[42:43] offset:2048 sc1\n"
    "global_load_lds_dwordx4 v5, s[42:43] offset:3072 sc1\n"
    "s_add_u32 s38, s38, 0x1000\n"
    "s_mov_b64 exec, 1\n"
    "v_mov_b32 v1, 46\n"
    "s_waitcnt vmcnt(16)\n"
    "ds_write_b32 v13, v1 offset:0\n"
    "s_mov_b32 m0, 110592\n"
    "s_add_u32 s42, s46, s38\n"
    "s_addc_u32 s43, s47, 0\n"
    "s_mov_b64 exec, -1\n"
    "global_load_lds_dwordx4 v5, s[42:43] offset:0 sc1\n"
    "global_load_lds_dwordx4 v5, s[42:43] offset:1024 sc1\n"
    "global_load_lds_dwordx4 v5, s[42:43] offset:2048 sc1\n"
    "global_load_lds_dwordx4 v5, s[42:43] offset:3072 sc1\n"
    "s_add_u32 s38, s38, 0x1000\n"
    "s_and_b32 s38, s38, 0x1ffff\n"
    "s_mov_b64 exec, 1\n"
    "v_mov_b32 v1, 48\n"
    "s_waitcnt vmcnt(16)\n"
    "ds_write_b32 v13, v1 offset:0\n"
    "s_mov_b32 m0, 114688\n"
    "s_add_u32 s42, s46, s38\n"
    "s_addc_u32 s43, s47, 0\n"
    "s_mov_b64 exec, -1\n"
    "global_load_lds_dwordx4 v5, s[42:43] offset:0 sc1\n"
    "global_load_lds_dwordx4 v5, s[42:43] offset:1024 sc1\n"
    "global_load_lds_dwordx4 v5, s[42:43] offset:2048 sc1\n"
    "global_load_lds_dwordx4 v5, s[42:43] offset:3072 sc1\n"
    "s_add_u32 s38, s38, 0x1000\n"
    "s_mov_b64 exec, 1\n"
    "v_mov_b32 v1, 50\n"
    "s_waitcnt vmcnt(16)\n"
    "ds_write_b32 v13, v1 offset:0\n"
    "s_mov_b32 m0, 118784\n"
    "s_add_u32 s42, s46, s38\n"
    "s_addc_u32 s43, s47, 0\n"
    "s_mov_b64 exec, -1\n"
    "global_load_lds_dwordx4 v5, s[42:43] offset:0 sc1\n"
    "global_load_lds_dwordx4 v5, s[42:43] offset:1024 sc1\n"
    "global_load_lds_dwordx4 v5, s[42:43] offset:2048 sc1\n"
    "global_load_lds_dwordx4 v5, s[42:43] offset:3072 sc1\n"
    "s_add_u32 s38, s38, 0x1000\n"
    "s_mov_b64 exec, 1\n"
    "v_mov_b32 v1, 52\n"
    "s_waitcnt vmcnt(16)\n"
    "ds_write_b32 v13, v1 offset:0\n"
    "s_mov_b32 m0, 122880\n"
    "s_add_u32 s42, s46, s38\n"
    "s_addc_u32 s43, s47, 0\n"
    "s_mov_b64 exec, -1\n"
    "global_load_lds_dwordx4 v5, s[42:43] offset:0 sc1\n"
    "global_load_lds_dwordx4 v5, s[42:43] offset:1024 sc1\n"
    "global_load_lds_dwordx4 v5, s[42:43] offset:2048 sc1\n"
    "global_load_lds_dwordx4 v5, s[42:43] offset:3072 sc1\n"
    "s_add_u32 s38, s38, 0x1000\n"
    "s_mov_b64 exec, 1\n"
    "v_mov_b32 v1, 54\n"
    "s_waitcnt vmcnt(16)\n"
    "ds_write_b32 v13, v1 offset:0\n"
    "s_mov_b32 m0, 126976\n"
    "s_add_u32 s42, s46, s38\n"
    "s_addc_u32 s43, s47, 0\n"
    "s_mov_b64 exec, -1\n"
    "global_load_lds_dwordx4 v5, s[42:43] offset:0 sc1\n"
    "global_load_lds_dwordx4 v5, s[42:43] offset:1024 sc1\n"
    "global_load_lds_dwordx4 v5, s[42:43] offset:2048 sc1\n"
    "global_load_lds_dwordx4 v5, s[42:43] offset:3072 sc1\n"
    "s_add_u32 s38, s38, 0x1000\n"
    "s_and_b32 s38, s38, 0x1ffff\n"
    "s_mov_b64 exec, 1\n"
    "v_mov_b32 v1, 56\n"
    "s_waitcnt vmcnt(16)\n"
    "ds_write_b32 v13, v1 offset:0\n"
    "v_mov_b32 v1, 60\n"
    "s_waitcnt vmcnt(8)\n"
    "ds_write_b32 v13, v1 offset:0\n"
    "v_mov_b32 v1, 64\n"
    "s_waitcnt vmcnt(0)\n"
    "ds_write_b32 v13, v1 offset:0\n"
    "s_mov_b64 exec, -1\n"
    ".Lwc0:\n"
    "s_lshl_b32 s40, s54, 12\n"
    "s_add_u32 s40, s40, 24227840\n"
    "v_lshl_add_u32 v4, v0, 7, s40\n"
    "s_mov_b32 exec_lo, 0x3fffff\n"
    "s_mov_b32 exec_hi, 0x0\n"
    "s_mov_b32 s61, 0\n"
    ".Lp_17:\n"
    "global_load_dwordx2 v[2:3], v4, s[10:11] sc1\n"
    "s_waitcnt vmcnt(0)\n"
    "v_cmp_ne_u64 vcc, s[34:35], v[2:3]\n"
    "s_add_u32 s61, s61, 1\n"
    "s_and_b64 exec, exec, vcc\n"
    "s_cbranch_scc0 .Ld_18\n"
    "s_sleep 2\n"
    "s_cmp_lt_u32 s61, 32768\n"
    "s_cbranch_scc1 .Lp_17\n"
    ".Ld_18:\n"
    "s_mov_b64 exec, -1\n"
    "s_waitcnt lgkmcnt(0)\n"
    "s_mov_b64 exec, 1\n"
    "v_mov_b32 v1, 1\n"
    "ds_add_u32 v13, v1 offset:184\n"
    "s_mov_b64 exec, -1\n"
    ".Lwc:\n"
    ".Lw_19:\n"
    "ds_read_b32 v1, v13 offset:184\n"
    "s_waitcnt lgkmcnt(0)\n"
    "v_readfirstlane_b32 s60, v1\n"
    "s_cmp_ge_u32 s60, 1\n"
    "s_cbranch_scc1 .Ld_20\n"
    "s_sleep 8\n"
    "s_add_u32 s63, s63, 1\n"
    "s_bitcmp1_b32 s63, 19\n"
    "s_cbranch_scc0 .Lw_19\n"
    "s_sub_u32 s63, s63, 1\n"
    ".Ld_20:\n"
    "s_and_b32 s41, s24, 1\n"
    "s_lshl1_add_u32 s40, s54, s41\n"
    "s_mul_i32 s40, s40, 90112\n"
    "s_add_u32 s40, s40, 16777216\n"
    "s_add_u32 s46, s10, s40\n"
    "s_addc_u32 s47, s11, 0\n"
    "s_lshl_b32 s50, s41, 12\n"
    "s_lshl_b32 s41, s41, 2\n"
    "v_add_u32 v9, s41, v13\n"
    "v_lshlrev_b32 v5, 4, v0\n"
    "v_mov_b32 v7, 0\n"
    "v_mov_b32 v8, 0\n"
    "s_cmp_eq_u32 s24, 12\n"
    "s_cbranch_scc0 .Lxt\n"
    ".Lw_21:\n"
    "ds_read_b32 v1, v13 offset:116\n"
    "s_waitcnt lgkmcnt(0)\n"
    "v_readfirstlane_b32 s60, v1\n"
    "s_cmp_ge_u32 s60, 1\n"
    "s_cbranch_scc1 .Ld_22\n"
    "s_sleep 8\n"
    "s_add_u32 s63, s63, 1\n"
    "s_bitcmp1_b32 s63, 19\n"
    "s_cbranch_scc0 .Lw_21\n"
    "s_sub_u32 s63, s63, 1\n"
    ".Ld_22:\n"
    "s_lshl_b32 s40, s53, 9\n"
    "s_add_u32 s40, s40, s54\n"
    "s_lshl_b32 s40, s40, 7\n"
    "v_lshl_add_u32 v1, v0, 10, s40\n"
    "global_load_dword v2, v1, s[4:5]\n"
    ".Lxt:\n"
    ".Lw_23:\n"
    "ds_read_b32 v1, v13 offset:116\n"
    "s_waitcnt lgkmcnt(0)\n"
    "v_readfirstlane_b32 s60, v1\n"
    "s_cmp_ge_u32 s60, 8\n"
    "s_cbranch_scc1 .Ld_24\n"
    "s_sleep 8\n"
    "s_add_u32 s63, s63, 1\n"
    "s_bitcmp1_b32 s63, 19\n"
    "s_cbranch_scc0 .Lw_23\n"
    "s_sub_u32 s63, s63, 1\n"
    ".Ld_24:\n"
    "s_add_u32 m0, s50, 24576\n"
    "s_add_u32 s42, s46, 32768\n"
    "s_addc_u32 s43, s47, 0\n"
    "s_mov_b64 exec, -1\n"
    "global_load_lds_dwordx4 v5, s[42:43] offset:0 sc1\n"
    "global_load_lds_dwordx4 v5, s[42:43] offset:1024 sc1\n"
    "global_load_lds_dwordx4 v5, s[42:43] offset:2048 sc1\n"
    "global_load_lds_dwordx4 v5, s[42:43] offset:3072 sc1\n"
    "s_mov_b64 exec, 1\n"
    "s_add_u32 m0, s50, 32768\n"
    "s_add_u32 s42, s46, 36864\n"
    "s_addc_u32 s43, s47, 0\n"
    "s_mov_b64 exec, -1\n"
    "global_load_lds_dwordx4 v5, s[42:43] offset:0 sc1\n"
    "global_load_lds_dwordx4 v5, s[42:43] offset:1024 sc1\n"
    "global_load_lds_dwordx4 v5, s[42:43] offset:2048 sc1\n"
    "global_load_lds_dwordx4 v5, s[42:43] offset:3072 sc1\n"
    "s_mov_b64 exec, 1\n"
    "s_add_u32 m0, s50, 40960\n"
    "s_add_u32 s42, s46, 40960\n"
    "s_addc_u32 s43, s47, 0\n"
    "s_mov_b64 exec, -1\n"
    "global_load_lds_dwordx4 v5, s[42:43] offset:0 sc1\n"
    "global_load_lds_dwordx4 v5, s[42:43] offset:1024 sc1\n"
    "global_load_lds_dwordx4 v5, s[42:43] offset:2048 sc1\n"
    "global_load_lds_dwordx4 v5, s[42:43] offset:3072 sc1\n"
    "s_mov_b64 exec, 1\n"
    "s_add_u32 m0, s50, 49152\n"
    "s_add_u32 s42, s46, 45056\n"
    "s_addc_u32 s43, s47, 0\n"
    "s_mov_b64 exec, -1\n"
    "global_load_lds_dwordx4 v5, s[42:43] offset:0 sc1\n"
    "global_load_lds_dwordx4 v5, s[42:43] offset:1024 sc1\n"
    "global_load_lds_dwordx4 v5, s[42:43] offset:2048 sc1\n"
    "global_load_lds_dwordx4 v5, s[42:43] offset:3072 sc1\n"
    "s_mov_b64 exec, 1\n"
    "s_add_u32 m0, s50, 57344\n"
    "s_add_u32 s42, s46, 49152\n"
    "s_addc_u32 s43, s47, 0\n"
    "s_mov_b64 exec, -1\n"
    "global_load_lds_dwordx4 v5, s[42:43] offset:0 sc1\n"
    "global_load_lds_dwordx4 v5, s[42:43] offset:1024 sc1\n"
    "global_load_lds_dwordx4 v5, s[42:43] offset:2048 sc1\n"
    "global_load_lds_dwordx4 v5, s[42:43] offset:3072 sc1\n"
    "s_mov_b64 exec, 1\n"
    "v_mov_b32 v7, 2\n"
    "s_waitcnt vmcnt(16)\n"
    "ds_write_b32 v9, v7 offset:160\n"
    "s_add_u32 m0, s50, 65536\n"
    "s_add_u32 s42, s46, 53248\n"
    "s_addc_u32 s43, s47, 0\n"
    "s_mov_b64 exec, -1\n"
    "global_load_lds_dwordx4 v5, s[42:43] offset:0 sc1\n"
    "global_load_lds_dwordx4 v5, s[42:43] offset:1024 sc1\n"
    "global_load_lds_dwordx4 v5, s[42:43] offset:2048 sc1\n"
    "global_load_lds_dwordx4 v5, s[42:43] offset:3072 sc1\n"
    "s_mov_b64 exec, 1\n"
    "v_mov_b32 v7, 4\n"
    "s_waitcnt vmcnt(16)\n"
    "ds_write_b32 v9, v7 offset:160\n"
    "s_add_u32 m0, s50, 73728\n"
    "s_add_u32 s42, s46, 57344\n"
    "s_addc_u32 s43, s47, 0\n"
    "s_mov_b64 exec, -1\n"
    "global_load_lds_dwordx4 v5, s[42:43] offset:0 sc1\n"
    "global_load_lds_dwordx4 v5, s[42:43] offset:1024 sc1\n"
    "global_load_lds_dwordx4 v5, s[42:43] offset:2048 sc1\n"
    "global_load_lds_dwordx4 v5, s[42:43] offset:3072 sc1\n"
    "s_mov_b64 exec, 1\n"
    "v_mov_b32 v7, 6\n"
    "s_waitcnt vmcnt(16)\n"
    "ds_write_b32 v9, v7 offset:160\n"
    "s_add_u32 m0, s50, 81920\n"
    "s_add_u32 s42, s46, 61440\n"
    "s_addc_u32 s43, s47, 0\n"
    "s_mov_b64 exec, -1\n"
    "global_load_lds_dwordx4 v5, s[42:43] offset:0 sc1\n"
    "global_load_lds_dwordx4 v5, s[42:43] offset:1024 sc1\n"
    "global_load_lds_dwordx4 v5, s[42:43] offset:2048 sc1\n"
    "global_load_lds_dwordx4 v5, s[42:43] offset:3072 sc1\n"
    "s_mov_b64 exec, 1\n"
    "v_mov_b32 v7, 8\n"
    "s_waitcnt vmcnt(16)\n"
    "ds_write_b32 v9, v7 offset:160\n"
    "s_add_u32 m0, s50, 90112\n"
    "s_add_u32 s42, s46, 65536\n"
    "s_addc_u32 s43, s47, 0\n"
    "s_mov_b64 exec, -1\n"
    "global_load_lds_dwordx4 v5, s[42:43] offset:0 sc1\n"
    "global_load_lds_dwordx4 v5, s[42:43] offset:1024 sc1\n"
    "global_load_lds_dwordx4 v5, s[42:43] offset:2048 sc1\n"
    "global_load_lds_dwordx4 v5, s[42:43] offset:3072 sc1\n"
    "s_mov_b64 exec, 1\n"
    "v_mov_b32 v7, 10\n"
    "s_waitcnt vmcnt(16)\n"
    "ds_write_b32 v9, v7 offset:160\n"
    "v_mov_b32 v7, 12\n"
    "s_waitcnt vmcnt(12)\n"
    "ds_write_b32 v9, v7 offset:160\n"
    "v_mov_b32 v7, 14\n"
    "s_waitcnt vmcnt(8)\n"
    "ds_write_b32 v9, v7 offset:160\n"
    "v_mov_b32 v7, 16\n"
    "s_waitcnt vmcnt(4)\n"
    "ds_write_b32 v9, v7 offset:160\n"
    "v_mov_b32 v7, 18\n"
    "s_waitcnt vmcnt(0)\n"
    "ds_write_b32 v9, v7 offset:160\n"
    "s_mov_b64 exec, -1\n"
    ".Lw_25:\n"
    "ds_read_b32 v1, v13 offset:132\n"
    "s_waitcnt lgkmcnt(0)\n"
    "v_readfirstlane_b32 s60, v1\n"
    "s_cmp_ge_u32 s60, 4\n"
    "s_cbranch_scc1 .Ld_26\n"
    "s_sleep 2\n"
    "s_add_u32 s63, s63, 1\n"
    "s_bitcmp1_b32 s63, 19\n"
    "s_cbranch_scc0 .Lw_25\n"
    "s_sub_u32 s63, s63, 1\n"
    ".Ld_26:\n"
    "s_add_u32 m0, s50, 131072\n"
    "s_add_u32 s42, s46, 69632\n"
    "s_addc_u32 s43, s47, 0\n"
    "s_mov_b64 exec, -1\n"
    "global_load_lds_dwordx4 v5, s[42:43] offset:0 sc1\n"
    "global_load_lds_dwordx4 v5, s[42:43] offset:1024 sc1\n"
    "global_load_lds_dwordx4 v5, s[42:43] offset:2048 sc1\n"
    "global_load_lds_dwordx4 v5, s[42:43] offset:3072 sc1\n"
    "s_mov_b64 exec, 1\n"
    "s_add_u32 m0, s50, 139264\n"
    "s_add_u32 s42, s46, 73728\n"
    "s_addc_u32 s43, s47, 0\n"
    "s_mov_b64 exec, -1\n"
    "global_load_lds_dwordx4 v5, s[42:43] offset:0 sc1\n"
    "global_load_lds_dwordx4 v5, s[42:43] offset:1024 sc1\n"
    "global_load_lds_dwordx4 v5, s[42:43] offset:2048 sc1\n"
    "global_load_lds_dwordx4 v5, s[42:43] offset:3072 sc1\n"
    "s_mov_b64 exec, 1\n"
    "s_add_u32 m0, s50, 147456\n"
    "s_add_u32 s42, s46, 77824\n"
    "s_addc_u32 s43, s47, 0\n"
    "s_mov_b64 exec, -1\n"
    "global_load_lds_dwordx4 v5, s[42:43] offset:0 sc1\n"
    "global_load_lds_dwordx4 v5, s[42:43] offset:1024 sc1\n"
    "global_load_lds_dwordx4 v5, s[42:43] offset:2048 sc1\n"
    "global_load_lds_dwordx4 v5, s[42:43] offset:3072 sc1\n"
    "s_mov_b64 exec, 1\n"
    "s_add_u32 m0, s50, 0\n"
    "s_add_u32 s42, s46, 81920\n"
    "s_addc_u32 s43, s47, 0\n"
    "s_mov_b64 exec, -1\n"
    "global_load_lds_dwordx4 v5, s[42:43] offset:0 sc1\n"
    "global_load_lds_dwordx4 v5, s[42:43] offset:1024 sc1\n"
    "global_load_lds_dwordx4 v5, s[42:43] offset:2048 sc1\n"
    "global_load_lds_dwordx4 v5, s[42:43] offset:3072 sc1\n"
    "s_mov_b64 exec, 1\n"
    "s_add_u32 m0, s50, 8192\n"
    "s_add_u32 s42, s46, 86016\n"
    "s_addc_u32 s43, s47, 0\n"
    "s_mov_b64 exec, -1\n"
    "global_load_lds_dwordx4 v5, s[42:43] offset:0 sc1\n"
    "global_load_lds_dwordx4 v5, s[42:43] offset:1024 sc1\n"
    "global_load_lds_dwordx4 v5, s[42:43] offset:2048 sc1\n"
    "global_load_lds_dwordx4 v5, s[42:43] offset:3072 sc1\n"
    "s_mov_b64 exec, 1\n"
    "v_mov_b32 v7, 20\n"
    "s_waitcnt vmcnt(16)\n"
    "ds_write_b32 v9, v7 offset:160\n"
    "s_add_u32 m0, s50, 16384\n"
    "s_add_u32 s42, s46, 0\n"
    "s_addc_u32 s43, s47, 0\n"
    "s_mov_b64 exec, -1\n"
    "global_load_lds_dwordx4 v5, s[42:43] offset:0 sc1\n"
    "global_load_lds_dwordx4 v5, s[42:43] offset:1024 sc1\n"
    "global_load_lds_dwordx4 v5, s[42:43] offset:2048 sc1\n"
    "global_load_lds_dwordx4 v5, s[42:43] offset:3072 sc1\n"
    "s_mov_b64 exec, 1\n"
    "v_mov_b32 v7, 22\n"
    "s_waitcnt vmcnt(16)\n"
    "ds_write_b32 v9, v7 offset:160\n"
    "v_mov_b32 v7, 24\n"
    "s_waitcnt vmcnt(12)\n"
    "ds_write_b32 v9, v7 offset:160\n"
    "v_mov_b32 v7, 26\n"
    "s_waitcnt vmcnt(8)\n"
    "ds_write_b32 v9, v7 offset:160\n"
    "v_mov_b32 v7, 28\n"
    "s_waitcnt vmcnt(4)\n"
    "ds_write_b32 v9, v7 offset:160\n"
    "v_mov_b32 v7, 30\n"
    "s_waitcnt vmcnt(0)\n"
    "ds_write_b32 v9, v7 offset:160\n"
    "s_mov_b64 exec, -1\n"
    ".Lw_27:\n"
    "ds_read_b32 v1, v13 offset:120\n"
    "s_waitcnt lgkmcnt(0)\n"
    "v_readfirstlane_b32 s60, v1\n"
    "s_cmp_ge_u32 s60, 8\n"
    "s_cbranch_scc1 .Ld_28\n"
    "s_sleep 2\n"
    "s_add_u32 s63, s63, 1\n"
    "s_bitcmp1_b32 s63, 19\n"
    "s_cbranch_scc0 .Lw_27\n"
    "s_sub_u32 s63, s63, 1\n"
    ".Ld_28:\n"
    "s_add_u32 m0, s50, 98304\n"
    "s_add_u32 s42, s46, 4096\n"
    "s_addc_u32 s43, s47, 0\n"
    "s_mov_b64 exec, -1\n"
    "global_load_lds_dwordx4 v5, s[42:43] offset:0 sc1\n"
    "global_load_lds_dwordx4 v5, s[42:43] offset:1024 sc1\n"
    "global_load_lds_dwordx4 v5, s[42:43] offset:2048 sc1\n"
    "global_load_lds_dwordx4 v5, s[42:43] offset:3072 sc1\n"
    "s_mov_b64 exec, 1\n"
    "s_add_u32 m0, s50, 106496\n"
    "s_add_u32 s42, s46, 8192\n"
    "s_addc_u32 s43, s47, 0\n"
    "s_mov_b64 exec, -1\n"
    "global_load_lds_dwordx4 v5, s[42:43] offset:0 sc1\n"
    "global_load_lds_dwordx4 v5, s[42:43] offset:1024 sc1\n"
    "global_load_lds_dwordx4 v5, s[42:43] offset:2048 sc1\n"
    "global_load_lds_dwordx4 v5, s[42:43] offset:3072 sc1\n"
    "s_mov_b64 exec, 1\n"
    "s_add_u32 m0, s50, 114688\n"
    "s_add_u32 s42, s46, 12288\n"
    "s_addc_u32 s43, s47, 0\n"
    "s_mov_b64 exec, -1\n"
    "global_load_lds_dwordx4 v5, s[42:43] offset:0 sc1\n"
    "global_load_lds_dwordx4 v5, s[42:43] offset:1024 sc1\n"
    "global_load_lds_dwordx4 v5, s[42:43] offset:2048 sc1\n"
    "global_load_lds_dwordx4 v5, s[42:43] offset:3072 sc1\n"
    "s_mov_b64 exec, 1\n"
    "s_add_u32 m0, s50, 122880\n"
    "s_add_u32 s42, s46, 16384\n"
    "s_addc_u32 s43, s47, 0\n"
    "s_mov_b64 exec, -1\n"
    "global_load_lds_dwordx4 v5, s[42:43] offset:0 sc1\n"
    "global_load_lds_dwordx4 v5, s[42:43] offset:1024 sc1\n"
    "global_load_lds_dwordx4 v5, s[42:43] offset:2048 sc1\n"
    "global_load_lds_dwordx4 v5, s[42:43] offset:3072 sc1\n"
    "s_mov_b64 exec, 1\n"
    "v_mov_b32 v7, 32\n"
    "s_waitcnt vmcnt(12)\n"
    "ds_write_b32 v9, v7 offset:160\n"
    "v_mov_b32 v7, 34\n"
    "s_waitcnt vmcnt(8)\n"
    "ds_write_b32 v9, v7 offset:160\n"
    "v_mov_b32 v7, 36\n"
    "s_waitcnt vmcnt(4)\n"
    "ds_write_b32 v9, v7 offset:160\n"
    "v_mov_b32 v7, 38\n"
    "s_waitcnt vmcnt(0)\n"
    "ds_write_b32 v9, v7 offset:160\n"
    "s_mov_b64 exec, -1\n"
    ".Lw_29:\n"
    "ds_read_b32 v1, v13 offset:148\n"
    "s_waitcnt lgkmcnt(0)\n"
    "v_readfirstlane_b32 s60, v1\n"
    "s_cmp_ge_u32 s60, 4\n"
    "s_cbranch_scc1 .Ld_30\n"
    "s_sleep 2\n"
    "s_add_u32 s63, s63, 1\n"
    "s_bitcmp1_b32 s63, 19\n"
    "s_cbranch_scc0 .Lw_29\n"
    "s_sub_u32 s63, s63, 1\n"
    ".Ld_30:\n"
    "s_add_u32 m0, s50, 24576\n"
    "s_add_u32 s42, s46, 20480\n"
    "s_addc_u32 s43, s47, 0\n"
    "s_mov_b64 exec, -1\n"
    "global_load_lds_dwordx4 v5, s[42:43] offset:0 sc1\n"
    "global_load_lds_dwordx4 v5, s[42:43] offset:1024 sc1\n"
    "global_load_lds_dwordx4 v5, s[42:43] offset:2048 sc1\n"
    "global_load_lds_dwordx4 v5, s[42:43] offset:3072 sc1\n"
    "s_mov_b64 exec, 1\n"
    "s_add_u32 m0, s50, 32768\n"
    "s_add_u32 s42, s46, 24576\n"
    "s_addc_u32 s43, s47, 0\n"
    "s_mov_b64 exec, -1\n"
    "global_load_lds_dwordx4 v5, s[42:43] offset:0 sc1\n"
    "global_load_lds_dwordx4 v5, s[42:43] offset:1024 sc1\n"
    "global_load_lds_dwordx4 v5, s[42:43] offset:2048 sc1\n"
    "global_load_lds_dwordx4 v5, s[42:43] offset:3072 sc1\n"
    "s_mov_b64 exec, 1\n"
    "s_add_u32 m0, s50, 40960\n"
    "s_add_u32 s42, s46, 28672\n"
    "s_addc_u32 s43, s47, 0\n"
    "s_mov_b64 exec, -1\n"
    "global_load_lds_dwordx4 v5, s[42:43] offset:0 sc1\n"
    "global_load_lds_dwordx4 v5, s[42:43] offset:1024 sc1\n"
    "global_load_lds_dwordx4 v5, s[42:43] offset:2048 sc1\n"
    "global_load_lds_dwordx4 v5, s[42:43] offset:3072 sc1\n"
    "s_mov_b64 exec, 1\n"
    "v_mov_b32 v7, 40\n"
    "s_waitcnt vmcnt(8)\n"
    "ds_write_b32 v9, v7 offset:160\n"
    "v_mov_b32 v7, 42\n"
    "s_waitcnt vmcnt(4)\n"
    "ds_write_b32 v9, v7 offset:160\n"
    "v_mov_b32 v7, 44\n"
    "s_waitcnt vmcnt(0)\n"
    "ds_write_b32 v9, v7 offset:160\n"
    "s_mov_b64 exec, -1\n"
    "s_endpgm\n"
    ".Lqz:\n"
    "s_lshr_b32 s40, s26, 2\n"
    "s_lshl3_add_u32 s40, s25, s40\n"
    "s_lshl_b32 s40, s40, 9\n"
    "s_add_u32 s40, s40, 23592960\n"
    "v_lshl_add_u32 v4, v0, 7, s40\n"
    "v_lshl_add_u32 v5, v0, 2, v13\n"
    "v_mov_b32 v6, 1\n"
    "s_sleep 16\n"
    ".Lw_31:\n"
    "ds_read_b32 v1, v13 offset:48\n"
    "s_waitcnt lgkmcnt(0)\n"
    "v_readfirstlane_b32 s60, v1\n"
    "s_cmp_ge_u32 s60, 4\n"
    "s_cbranch_scc1 .Ld_32\n"
    "s_sleep 1\n"
    "s_add_u32 s63, s63, 1\n"
    "s_bitcmp1_b32 s63, 19\n"
    "s_cbranch_scc0 .Lw_31\n"
    "s_sub_u32 s63, s63, 1\n"
    ".Ld_32:\n"
    "s_mov_b64 s[44:45], 15\n"
    "s_mov_b32 s37, 0\n"
    ".Lq_l:\n"
    "s_mov_b64 exec, s[44:45]\n"
    "global_load_dwordx2 v[2:3], v4, s[10:11] sc1\n"
    "s_waitcnt vmcnt(0)\n"
    "v_cmp_ne_u64 vcc, s[34:35], v[2:3]\n"
    "s_add_u32 s37, s37, 1\n"
    "s_cmp_lt_u32 s37, 32768\n"
    "s_cselect_b64 s[44:45], vcc, 0\n"
    "s_andn2_b64 exec, exec, s[44:45]\n"
    "s_cbranch_scc0 .Lq_s\n"
    "ds_write_b32 v5, v6 offset:80\n"
    "s_cmp_eq_u64 s[44:45], 0\n"
    "s_cbranch_scc1 .Lq_d\n"
    ".Lq_s:\n"
    "s_sleep 2\n"
    "s_branch .Lq_l\n"
    ".Lq_d:\n"
    "s_mov_b64 exec, -1\n"
    "s_branch .Lwld\n"
    ".Lq_o:\n"
    "s_lshl_b32 s40, s53, 13\n"
    "s_add_u32 s40, s40, 23965696\n"
    "v_lshl_add_u32 v4, v0, 7, s40\n"
    ".Lw_33:\n"
    "ds_read_b32 v1, v13 offset:116\n"
    "s_waitcnt lgkmcnt(0)\n"
    "v_readfirstlane_b32 s60, v1\n"
    "s_cmp_ge_u32 s60, 8\n"
    "s_cbranch_scc1 .Ld_34\n"
    "s_sleep 8\n"
    "s_add_u32 s63, s63, 1\n"
    "s_bitcmp1_b32 s63, 19\n"
    "s_cbranch_scc0 .Lw_33\n"
    "s_sub_u32 s63, s63, 1\n"
    ".Ld_34:\n"
    "s_sleep 32\n"
    "s_mov_b64 exec, -1\n"
    "s_mov_b32 s61, 0\n"
    ".Lp_35:\n"
    "global_load_dwordx2 v[2:3], v4, s[10:11] sc1\n"
    "s_waitcnt vmcnt(0)\n"
    "v_cmp_ne_u64 vcc, s[34:35], v[2:3]\n"
    "s_add_u32 s61, s61, 1\n"
    "s_and_b64 exec, exec, vcc\n"
    "s_cbranch_scc0 .Ld_36\n"
    "s_sleep 16\n"
    "s_cmp_lt_u32 s61, 32768\n"
    "s_cbranch_scc1 .Lp_35\n"
    ".Ld_36:\n"
    "s_mov_b64 exec, -1\n"
    "v_lshrrev_b32 v1, 5, v0\n"
    "v_lshlrev_b32 v1, 15, v1\n"
    "v_and_b32 v2, 30, v0\n"
    "v_lshl_or_b32 v1, v2, 10, v1\n"
    "v_and_b32 v2, 1, v0\n"
    "v_lshl_or_b32 v1, v2, 9, v1\n"
    "s_lshl_b32 s40, s53, 16\n"
    "s_add_u32 s40, s40, 1532\n"
    "v_add_u32 v4, s40, v1\n"
    ".Lw_37:\n"
    "ds_read_b32 v1, v13 offset:120\n"
    "s_waitcnt lgkmcnt(0)\n"
    "v_readfirstlane_b32 s60, v1\n"
    "s_cmp_ge_u32 s60, 8\n"
    "s_cbranch_scc1 .Ld_38\n"
    "s_sleep 2\n"
    "s_add_u32 s63, s63, 1\n"
    "s_bitcmp1_b32 s63, 19\n"
    "s_cbranch_scc0 .Lw_37\n"
    "s_sub_u32 s63, s63, 1\n"
    ".Ld_38:\n"
    "s_waitcnt lgkmcnt(0)\n"
    "s_mov_b64 exec, 1\n"
    "v_mov_b32 v1, 1\n"
    "ds_add_u32 v13, v1 offset:140\n"
    "s_mov_b64 exec, -1\n"
    "s_endpgm\n"
    ".Lsync:\n"
    "s_cmp_lt_u32 s26, 28\n"
    "s_cbranch_scc0 .Ls_sp\n"
    "v_lshrrev_b32 v1, 1, v0\n"
    "v_lshlrev_b32 v1, 15, v1\n"
    "v_and_b32 v2, 1, v0\n"
    "v_lshl_or_b32 v1, v2, 10, v1\n"
    "s_lshl_b32 s40, s30, 11\n"
    "s_add_u32 s40, s40, 23135228\n"
    "v_add_u32 v4, s40, v1\n"
    "s_sleep 40\n"
    ".Lw_39:\n"
    "ds_read_b32 v1, v13 offset:64\n"
    "s_waitcnt lgkmcnt(0)\n"
    "v_readfirstlane_b32 s60, v1\n"
    "s_cmp_ge_u32 s60, 4\n"
    "s_cbranch_scc1 .Ld_40\n"
    "s_sleep 1\n"
    "s_add_u32 s63, s63, 1\n"
    "s_bitcmp1_b32 s63, 19\n"
    "s_cbranch_scc0 .Lw_39\n"
    "s_sub_u32 s63, s63, 1\n"
    ".Ld_40:\n"
    "s_mov_b32 exec_lo, 0xfffffff\n"
    "s_mov_b32 exec_hi, 0x0\n"
    "s_mov_b32 s61, 0\n"
    ".Lp_41:\n"
    "global_load_dword v2, v4, s[10:11] sc1\n"
    "s_waitcnt vmcnt(0)\n"
    "v_cmp_ne_u32 vcc, s35, v2\n"
    "s_add_u32 s61, s61, 1\n"
    "s_and_b64 exec, exec, vcc\n"
    "s_cbranch_scc0 .Ld_42\n"
    "s_sleep 2\n"
    "s_cmp_lt_u32 s61, 32768\n"
    "s_cbranch_scc1 .Lp_41\n"
    ".Ld_42:\n"
    "s_mov_b64 exec, -1\n"
    "s_waitcnt lgkmcnt(0)\n"
    "s_mov_b64 exec, 1\n"
    "v_mov_b32 v1, 1\n"
    "ds_add_u32 v13, v1 offset:96\n"
    "s_mov_b64 exec, -1\n"
    ".Lw_43:\n"
    "ds_read_b32 v1, v13 offset:104\n"
    "s_waitcnt lgkmcnt(0)\n"
    "v_readfirstlane_b32 s60, v1\n"
    "s_cmp_ge_u32 s60, 2\n"
    "s_cbranch_scc1 .Ld_44\n"
    "s_sleep 1\n"
    "s_add_u32 s63, s63, 1\n"
    "s_bitcmp1_b32 s63, 19\n"
    "s_cbranch_scc0 .Lw_43\n"
    "s_sub_u32 s63, s63, 1\n"
    ".Ld_44:\n"
    "v_lshlrev_b32 v8, 4, v0\n"
    "v_add_u32 v4, 81920, v8\n"
    "ds_read_b128 v[16:19], v4 offset:0\n"
    "ds_read_b128 v[20:23], v4 offset:1024\n"
    "ds_read_b128 v[24:27], v4 offset:2048\n"
    "ds_read_b128 v[28:31], v4 offset:3072\n"
    "ds_read_b128 v[32:35], v4 offset:4096\n"
    "ds_read_b128 v[36:39], v4 offset:5120\n"
    "ds_read_b128 v[40:43], v4 offset:6144\n"
    "ds_read_b128 v[44:47], v4 offset:7168\n"
    "ds_read_b128 v[48:51], v4 offset:8192\n"
    "ds_read_b128 v[52:55], v4 offset:9216\n"
    "ds_read_b128 v[56:59], v4 offset:10240\n"
    "ds_read_b128 v[60:63], v4 offset:11264\n"
    "ds_read_b128 v[64:67], v4 offset:12288\n"
    "ds_read_b128 v[68:71], v4 offset:13312\n"
    "ds_read_b128 v[72:75], v4 offset:14336\n"
    "ds_read_b128 v[76:79], v4 offset:15360\n"
    "s_waitcnt lgkmcnt(0)\n"
    "s_mov_b64 exec, 1\n"
    "v_mov_b32 v1, 1\n"
    "ds_add_u32 v13, v1 offset:108\n"
    "s_mov_b64 exec, -1\n"
    "s_lshl_b32 s62, s25, 2\n"
    "s_lshl_b32 s59, s27, 1\n"
    "s_xor_b32 s59, s59, s28\n"
    "s_lshl_b32 s59, s59, 1\n"
    "s_and_b32 s38, s59, 12\n"
    "s_lshl_b32 s38, s38, 1\n"
    "v_xor_b32 v1, s38, v0\n"
    "s_lshl_b32 s38, s62, 1\n"
    "v_add_u32 v1, s38, v1\n"
    "v_and_b32 v1, 63, v1\n"
    "s_lshl_b32 s40, s28, 14\n"
    "s_add_u32 s40, s40, 23719936\n"
    "v_lshl_add_u32 v4, v1, 7, s40\n"
    "v_add_u32 v5, 8192, v4\n"
    ".Lw_45:\n"
    "ds_read_b32 v1, v13 offset:100\n"
    "s_waitcnt lgkmcnt(0)\n"
    "v_readfirstlane_b32 s60, v1\n"
    "s_cmp_ge_u32 s60, 4\n"
    "s_cbranch_scc1 .Ld_46\n"
    "s_sleep 1\n"
    "s_add_u32 s63, s63, 1\n"
    "s_bitcmp1_b32 s63, 19\n"
    "s_cbranch_scc0 .Lw_45\n"
    "s_sub_u32 s63, s63, 1\n"
    ".Ld_46:\n"
    "s_mov_b64 s[46:47], -1\n"
    "s_bfm_b64 exec, 16, 0\n"
    "s_mov_b32 s37, 0\n"
    ".Lzr_l:\n"
    "global_load_dwordx2 v[2:3], v4, s[10:11] sc1\n"
    "global_load_dwordx2 v[6:7], v5, s[10:11] sc1\n"
    "s_add_u32 s37, s37, 1\n"
    "s_waitcnt vmcnt(0)\n"
    "v_cmp_ne_u64 vcc, s[34:35], v[2:3]\n"
    "v_cmp_ne_u64 s[44:45], s[34:35], v[6:7]\n"
    "s_or_b64 s[44:45], s[44:45], vcc\n"
    "s_andn2_b64 s[46:47], s[46:47], exec\n"
    "s_or_b64 s[44:45], s[44:45], s[46:47]\n"
    "s_mov_b64 s[46:47], s[44:45]\n"
    "s_cmp_lt_u32 s37, 32768\n"
    "s_cselect_b64 s[44:45], s[44:45], 0\n"
    "s_ff1_i32_b64 s40, s[44:45]\n"
    "s_lshr_b32 s40, s40, 1\n"
    "s_min_u32 s40, s40, 32\n"
    "v_mov_b32 v1, s40\n"
    "ds_write_b32 v13, v1 offset:68\n"
    "s_cmp_eq_u32 s40, 32\n"
    "s_cbranch_scc1 .Lzr_d\n"
    "s_lshl_b32 s40, s40, 1\n"
    "s_bfm_b64 exec, 16, s40\n"
    "s_and_b64 exec, exec, s[44:45]\n"
    "s_sleep 2\n"
    "s_branch .Lzr_l\n"
    ".Lzr_d:\n"
    "s_mov_b64 exec, -1\n"
    "s_cmp_eq_u32 s33, 0\n"
    "s_cbranch_scc0 .Ls_e\n"
    "v_mov_b32 v2, s34\n"
    "v_mov_b32 v3, s35\n"
    "v_mov_b32 v4, 24260608\n"
    "s_waitcnt vmcnt(0)\n"
    "s_mov_b64 exec, 1\n"
    "global_store_dwordx2 v4, v[2:3], s[10:11] sc1\n"
    "s_mov_b64 exec, -1\n"
    ".Ls_e:\n"
    "s_mul_i32 s40, s30, 28\n"
    "s_add_u32 s40, s40, s31\n"
    "s_lshl_b32 s40, s40, 13\n"
    "s_add_u32 s40, s40, 18878464\n"
    "s_add_u32 s42, s10, s40\n"
    "s_addc_u32 s43, s11, 0\n"
    "s_add_u32 s44, s42, 114688\n"
    "s_addc_u32 s45, s43, 0\n"
    ".Lw_47:\n"
    "ds_read_b32 v1, v13 offset:16\n"
    "s_waitcnt lgkmcnt(0)\n"
    "v_readfirstlane_b32 s60, v1\n"
    "s_cmp_ge_u32 s60, 12\n"
    "s_cbranch_scc1 .Ld_48\n"
    "s_sleep 8\n"
    "s_add_u32 s63, s63, 1\n"
    "s_bitcmp1_b32 s63, 19\n"
    "s_cbranch_scc0 .Lw_47\n"
    "s_sub_u32 s63, s63, 1\n"
    ".Ld_48:\n"
    "global_store_dwordx4 v8, v[16:19], s[42:43] offset:-4096 sc1\n"
    "global_store_dwordx4 v8, v[20:23], s[42:43] offset:-3072 sc1\n"
    "global_store_dwordx4 v8, v[24:27], s[42:43] offset:-2048 sc1\n"
    "global_store_dwordx4 v8, v[28:31], s[42:43] offset:-1024 sc1\n"
    "global_store_dwordx4 v8, v[32:35], s[42:43] offset:0 sc1\n"
    "global_store_dwordx4 v8, v[36:39], s[42:43] offset:1024 sc1\n"
    "global_store_dwordx4 v8, v[40:43], s[42:43] offset:2048 sc1\n"
    "global_store_dwordx4 v8, v[44:47], s[42:43] offset:3072 sc1\n"
    "global_store_dwordx4 v8, v[48:51], s[44:45] offset:-4096 sc1\n"
    "global_store_dwordx4 v8, v[52:55], s[44:45] offset:-3072 sc1\n"
    "global_store_dwordx4 v8, v[56:59], s[44:45] offset:-2048 sc1\n"
    "global_store_dwordx4 v8, v[60:63], s[44:45] offset:-1024 sc1\n"
    "global_store_dwordx4 v8, v[64:67], s[44:45] offset:0 sc1\n"
    "global_store_dwordx4 v8, v[68:71], s[44:45] offset:1024 sc1\n"
    "global_store_dwordx4 v8, v[72:75], s[44:45] offset:2048 sc1\n"
    "global_store_dwordx4 v8, v[76:79], s[44:45] offset:3072 sc1\n"
    "s_lshl4_add_u32 s41, s31, s30\n"
    "s_lshl_b32 s41, s41, 7\n"
    "s_add_u32 s41, s41, 23691264\n"
    "v_mov_b32 v2, s34\n"
    "v_mov_b32 v3, s35\n"
    "v_lshl_add_u32 v4, v0, 3, s41\n"
    "s_waitcnt vmcnt(0)\n"
    "s_mov_b64 exec, 0xffff\n"
    "global_store_dwordx2 v4, v[2:3], s[10:11] sc1\n"
    "s_mov_b64 exec, -1\n"
    "s_lshl_b32 s40, s28, 5\n"
    "s_lshr_b32 s41, s52, 1\n"
    "s_add_u32 s40, s40, s41\n"
    "s_lshl_b32 s40, s40, 7\n"
    "s_add_u32 s40, s40, 23691264\n"
    "v_lshl_add_u32 v4, v0, 11, s40\n"
    ".Lw_49:\n"
    "ds_read_b32 v1, v13 offset:16\n"
    "s_waitcnt lgkmcnt(0)\n"
    "v_readfirstlane_b32 s60, v1\n"
    "s_cmp_ge_u32 s60, 20\n"
    "s_cbranch_scc1 .Ld_50\n"
    "s_sleep 8\n"
    "s_add_u32 s63, s63, 1\n"
    "s_bitcmp1_b32 s63, 19\n"
    "s_cbranch_scc0 .Lw_49\n"
    "s_sub_u32 s63, s63, 1\n"
    ".Ld_50:\n"
    "s_mov_b64 exec, 3\n"
    "s_mov_b32 s61, 0\n"
    ".Lp_51:\n"
    "global_load_dwordx2 v[2:3], v4, s[10:11] sc1\n"
    "s_waitcnt vmcnt(0)\n"
    "v_cmp_ne_u64 vcc, s[34:35], v[2:3]\n"
    "s_add_u32 s61, s61, 1\n"
    "s_and_b64 exec, exec, vcc\n"
    "s_cbranch_scc0 .Ld_52\n"
    "s_sleep 2\n"
    "s_cmp_lt_u32 s61, 32768\n"
    "s_cbranch_scc1 .Lp_51\n"
    ".Ld_52:\n"
    "s_mov_b64 exec, -1\n"
    "s_waitcnt lgkmcnt(0)\n"
    "s_mov_b64 exec, 1\n"
    "v_mov_b32 v1, 1\n"
    "ds_add_u32 v13, v1 offset:112\n"
    "s_mov_b64 exec, -1\n"
    ".Ls_sp:\n"
    "s_lshl_b32 s40, s53, 12\n"
    "s_add_u32 s40, s40, 23834624\n"
    "v_lshl_add_u32 v4, v0, 7, s40\n"
    ".Lw_53:\n"
    "ds_read_b32 v1, v13 offset:116\n"
    "s_waitcnt lgkmcnt(0)\n"
    "v_readfirstlane_b32 s60, v1\n"
    "s_cmp_ge_u32 s60, 1\n"
    "s_cbranch_scc1 .Ld_54\n"
    "s_sleep 8\n"
    "s_add_u32 s63, s63, 1\n"
    "s_bitcmp1_b32 s63, 19\n"
    "s_cbranch_scc0 .Lw_53\n"
    "s_sub_u32 s63, s63, 1\n"
    ".Ld_54:\n"
    ".Lw_55:\n"
    "ds_read_b32 v1, v13 offset:132\n"
    "s_waitcnt lgkmcnt(0)\n"
    "v_readfirstlane_b32 s60, v1\n"
    "s_cmp_ge_u32 s60, 4\n"
    "s_cbranch_scc1 .Ld_56\n"
    "s_sleep 2\n"
    "s_add_u32 s63, s63, 1\n"
    "s_bitcmp1_b32 s63, 19\n"
    "s_cbranch_scc0 .Lw_55\n"
    "s_sub_u32 s63, s63, 1\n"
    ".Ld_56:\n"
    ".Lw_57:\n"
    "ds_read_b64 v[6:7], v13 offset:160\n"
    "s_waitcnt lgkmcnt(0)\n"
    "v_min_u32 v1, v6, v7\n"
    "s_nop 0\n"
    "v_readfirstlane_b32 s60, v1\n"
    "s_cmp_ge_u32 s60, 16\n"
    "s_cbranch_scc1 .Ld_58\n"
    "s_sleep 2\n"
    "s_add_u32 s63, s63, 1\n"
    "s_bitcmp1_b32 s63, 19\n"
    "s_cbranch_scc0 .Lw_57\n"
    "s_sub_u32 s63, s63, 1\n"
    ".Ld_58:\n"
    "s_mov_b32 exec_lo, 0xfffffff\n"
    "s_mov_b32 exec_hi, 0x0\n"
    "s_mov_b32 s61, 0\n"
    ".Lp_59:\n"
    "global_load_dwordx2 v[2:3], v4, s[10:11] sc1\n"
    "s_waitcnt vmcnt(0)\n"
    "v_cmp_ne_u64 vcc, s[34:35], v[2:3]\n"
    "s_add_u32 s61, s61, 1\n"
    "s_and_b64 exec, exec, vcc\n"
    "s_cbranch_scc0 .Ld_60\n"
    "s_sleep 2\n"
    "s_cmp_lt_u32 s61, 32768\n"
    "s_cbranch_scc1 .Lp_59\n"
    ".Ld_60:\n"
    "s_mov_b64 exec, -1\n"
    "s_waitcnt lgkmcnt(0)\n"
    "s_mov_b64 exec, 1\n"
    "v_mov_b32 v1, 1\n"
    "ds_add_u32 v13, v1 offset:136\n"
    "s_mov_b64 exec, -1\n"
    "s_endpgm\n"
    ".Lwld:\n"
    "s_cmp_lt_u32 s26, 28\n"
    "s_cbranch_scc0 .Lquit\n"
    "v_lshrrev_b32 v1, 3, v0\n"
    "v_and_b32 v2, 7, v0\n"
    "v_lshrrev_b32 v5, 4, v0\n"
    "v_xor_b32 v2, v2, v5\n"
    "v_lshlrev_b32 v2, 4, v2\n"
    "v_lshl_or_b32 v4, v1, 12, v2\n"
    "v_xor_b32 v5, 64, v4\n"
    "v_add_u32 v5, 0x8000, v5\n"
    "v_mov_b32 v7, 0\n"
    "s_bfe_u32 s41, s24, 0x10001\n"
    "s_lshl1_add_u32 s40, s52, s41\n"
    "s_lshl_b32 s40, s40, 17\n"
    "s_lshl_b32 s47, s41, 12\n"
    "s_lshl_b32 s41, s41, 2\n"
    "v_add_u32 v9, s41, v13\n"
    "s_mov_b32 s39, 0\n"
    "s_mov_b32 s44, 3\n"
    "s_add_u32 s40, s16, s40\n"
    "s_addc_u32 s41, s17, 0\n"
    "s_lshl_b32 s62, s25, 2\n"
    "s_lshl_b32 s59, s27, 1\n"
    "s_xor_b32 s59, s59, s28\n"
    "s_lshl_b32 s59, s59, 1\n"
    "s_mov_b32 s38, 3\n"
    ".Lw_61:\n"
    "ds_read_b32 v1, v13 offset:152\n"
    "s_waitcnt lgkmcnt(0)\n"
    "v_readfirstlane_b32 s60, v1\n"
    "s_cmp_ge_u32 s60, 1\n"
    "s_cbranch_scc1 .Ld_62\n"
    "s_sleep 8\n"
    "s_add_u32 s63, s63, 1\n"
    "s_bitcmp1_b32 s63, 19\n"
    "s_cbranch_scc0 .Lw_61\n"
    "s_sub_u32 s63, s63, 1\n"
    ".Ld_62:\n"
    ".Lw_63:\n"
    "ds_read_b32 v1, v13 offset:100\n"
    "s_waitcnt lgkmcnt(0)\n"
    "v_readfirstlane_b32 s60, v1\n"
    "s_cmp_ge_u32 s60, 4\n"
    "s_cbranch_scc1 .Ld_64\n"
    "s_sleep 4\n"
    "s_add_u32 s63, s63, 1\n"
    "s_bitcmp1_b32 s63, 19\n"
    "s_cbranch_scc0 .Lw_63\n"
    "s_sub_u32 s63, s63, 1\n"
    ".Ld_64:\n"
    "s_add_u32 s44, s44, 1\n"
    "s_add_u32 s46, s44, 3\n"
    "s_and_b32 s46, s46, -4\n"
    "s_min_u32 s46, s46, 32\n"
    "s_cmp_ge_u32 s39, s46\n"
    "s_cbranch_scc1 .Lz_65\n"
    ".Lw_66:\n"
    "ds_read_b32 v1, v13 offset:68\n"
    "s_waitcnt lgkmcnt(0)\n"
    "v_readfirstlane_b32 s39, v1\n"
    "s_cmp_ge_u32 s39, s46\n"
    "s_cbranch_scc1 .Ld_67\n"
    "s_sleep 1\n"
    "s_add_u32 s63, s63, 1\n"
    "s_bitcmp1_b32 s63, 19\n"
    "s_cbranch_scc0 .Lw_66\n"
    "s_sub_u32 s63, s63, 1\n"
    ".Ld_67:\n"
    ".Lz_65:\n"
    "v_add_u32 v7, 1, v7\n"
    "s_waitcnt vmcnt(0)\n"
    "ds_write_b32 v9, v7 offset:16\n"
    "v_and_b32 v1, 7, v0\n"
    "v_lshl_add_u32 v10, v1, 2, v13\n"
    ".Lw_a:\n"
    "ds_read_b32 v1, v10 offset:192\n"
    "s_waitcnt lgkmcnt(0)\n"
    "v_cmp_gt_u32 vcc, 24576, v1\n"
    "s_cmp_eq_u64 vcc, 0\n"
    "s_cbranch_scc1 .Lw_b\n"
    "s_sleep 2\n"
    "s_add_u32 s63, s63, 1\n"
    "s_bitcmp1_b32 s63, 19\n"
    "s_cbranch_scc0 .Lw_a\n"
    "s_sub_u32 s63, s63, 1\n"
    ".Lw_b:\n"
    "s_xor_b32 s45, 3, s59\n"
    "s_add_u32 s45, s45, s62\n"
    "s_and_b32 s45, s45, 31\n"
    "s_lshl_b32 s45, s45, 7\n"
    "s_add_u32 m0, s47, 0\n"
    "s_add_u32 s42, s40, s45\n"
    "s_addc_u32 s43, s41, 0\n"
    "global_load_lds_dwordx4 v4, s[42:43]\n"
    "s_add_u32 m0, m0, 0x400\n"
    "s_nop 0\n"
    "global_load_lds_dwordx4 v5, s[42:43]\n"
    "s_add_u32 m0, m0, 0x400\n"
    "s_add_u32 s42, s42, 0x10000\n"
    "s_addc_u32 s43, s43, 0\n"
    "global_load_lds_dwordx4 v4, s[42:43]\n"
    "s_add_u32 m0, m0, 0x400\n"
    "s_nop 0\n"
    "global_load_lds_dwordx4 v5, s[42:43]\n"
    "s_add_u32 s44, s44, 1\n"
    "s_add_u32 s46, s44, 3\n"
    "s_and_b32 s46, s46, -4\n"
    "s_min_u32 s46, s46, 32\n"
    "s_cmp_ge_u32 s39, s46\n"
    "s_cbranch_scc1 .Lz_68\n"
    ".Lw_69:\n"
    "ds_read_b32 v1, v13 offset:68\n"
    "s_waitcnt lgkmcnt(0)\n"
    "v_readfirstlane_b32 s39, v1\n"
    "s_cmp_ge_u32 s39, s46\n"
    "s_cbranch_scc1 .Ld_70\n"
    "s_sleep 1\n"
    "s_add_u32 s63, s63, 1\n"
    "s_bitcmp1_b32 s63, 19\n"
    "s_cbranch_scc0 .Lw_69\n"
    "s_sub_u32 s63, s63, 1\n"
    ".Ld_70:\n"
    ".Lz_68:\n"
    "v_add_u32 v7, 1, v7\n"
    "s_waitcnt vmcnt(4)\n"
    "ds_write_b32 v9, v7 offset:16\n"
    "s_xor_b32 s45, 4, s59\n"
    "s_add_u32 s45, s45, s62\n"
    "s_and_b32 s45, s45, 31\n"
    "s_lshl_b32 s45, s45, 7\n"
    "s_add_u32 m0, s47, 8192\n"
    "s_add_u32 s42, s40, s45\n"
    "s_addc_u32 s43, s41, 0\n"
    "global_load_lds_dwordx4 v4, s[42:43]\n"
    "s_add_u32 m0, m0, 0x400\n"
    "s_nop 0\n"
    "global_load_lds_dwordx4 v5, s[42:43]\n"
    "s_add_u32 m0, m0, 0x400\n"
    "s_add_u32 s42, s42, 0x10000\n"
    "s_addc_u32 s43, s43, 0\n"
    "global_load_lds_dwordx4 v4, s[42:43]\n"
    "s_add_u32 m0, m0, 0x400\n"
    "s_nop 0\n"
    "global_load_lds_dwordx4 v5, s[42:43]\n"
    "s_add_u32 s44, s44, 1\n"
    "s_add_u32 s46, s44, 3\n"
    "s_and_b32 s46, s46, -4\n"
    "s_min_u32 s46, s46, 32\n"
    "s_cmp_ge_u32 s39, s46\n"
    "s_cbranch_scc1 .Lz_71\n"
    ".Lw_72:\n"
    "ds_read_b32 v1, v13 offset:68\n"
    "s_waitcnt lgkmcnt(0)\n"
    "v_readfirstlane_b32 s39, v1\n"
    "s_cmp_ge_u32 s39, s46\n"
    "s_cbranch_scc1 .Ld_73\n"
    "s_sleep 1\n"
    "s_add_u32 s63, s63, 1\n"
    "s_bitcmp1_b32 s63, 19\n"
    "s_cbranch_scc0 .Lw_72\n"
    "s_sub_u32 s63, s63, 1\n"
    ".Ld_73:\n"
    ".Lz_71:\n"
    "v_add_u32 v7, 1, v7\n"
    "s_waitcnt vmcnt(8)\n"
    "ds_write_b32 v9, v7 offset:16\n"
    "s_xor_b32 s45, 5, s59\n"
    "s_add_u32 s45, s45, s62\n"
    "s_and_b32 s45, s45, 31\n"
    "s_lshl_b32 s45, s45, 7\n"
    "s_add_u32 m0, s47, 16384\n"
    "s_add_u32 s42, s40, s45\n"
    "s_addc_u32 s43, s41, 0\n"
    "global_load_lds_dwordx4 v4, s[42:43]\n"
    "s_add_u32 m0, m0, 0x400\n"
    "s_nop 0\n"
    "global_load_lds_dwordx4 v5, s[42:43]\n"
    "s_add_u32 m0, m0, 0x400\n"
    "s_add_u32 s42, s42, 0x10000\n"
    "s_addc_u32 s43, s43, 0\n"
    "global_load_lds_dwordx4 v4, s[42:43]\n"
    "s_add_u32 m0, m0, 0x400\n"
    "s_nop 0\n"
    "global_load_lds_dwordx4 v5, s[42:43]\n"
    "s_add_u32 s44, s44, 1\n"
    "s_add_u32 s46, s44, 3\n"
    "s_and_b32 s46, s46, -4\n"
    "s_min_u32 s46, s46, 32\n"
    "s_cmp_ge_u32 s39, s46\n"
    "s_cbranch_scc1 .Lz_74\n"
    ".Lw_75:\n"
    "ds_read_b32 v1, v13 offset:68\n"
    "s_waitcnt lgkmcnt(0)\n"
    "v_readfirstlane_b32 s39, v1\n"
    "s_cmp_ge_u32 s39, s46\n"
    "s_cbranch_scc1 .Ld_76\n"
    "s_sleep 1\n"
    "s_add_u32 s63, s63, 1\n"
    "s_bitcmp1_b32 s63, 19\n"
    "s_cbranch_scc0 .Lw_75\n"
    "s_sub_u32 s63, s63, 1\n"
    ".Ld_76:\n"
    ".Lz_74:\n"
    "v_add_u32 v7, 1, v7\n"
    "s_waitcnt vmcnt(8)\n"
    "ds_write_b32 v9, v7 offset:16\n"
    "s_mov_b32 s38, 6\n"
    "s_mov_b32 s36, 0\n"
    "s_mov_b32 s37, 4\n"
    ".Lw_l:\n"
    ".Lw_77:\n"
    "ds_read_b32 v1, v13 offset:24\n"
    "s_waitcnt lgkmcnt(0)\n"
    "v_readfirstlane_b32 s60, v1\n"
    "s_cmp_ge_u32 s60, s37\n"
    "s_cbranch_scc1 .Ld_78\n"
    "s_sleep 1\n"
    "s_add_u32 s63, s63, 1\n"
    "s_bitcmp1_b32 s63, 19\n"
    "s_cbranch_scc0 .Lw_77\n"
    "s_sub_u32 s63, s63, 1\n"
    ".Ld_78:\n"
    "s_xor_b32 s45, s38, s59\n"
    "s_add_u32 s45, s45, s62\n"
    "s_and_b32 s45, s45, 31\n"
    "s_lshl_b32 s45, s45, 7\n"
    "s_add_u32 m0, s47, 131072\n"
    "s_add_u32 s42, s40, s45\n"
    "s_addc_u32 s43, s41, 0\n"
    "global_load_lds_dwordx4 v4, s[42:43]\n"
    "s_add_u32 m0, m0, 0x400\n"
    "s_nop 0\n"
    "global_load_lds_dwordx4 v5, s[42:43]\n"
    "s_add_u32 m0, m0, 0x400\n"
    "s_add_u32 s42, s42, 0x10000\n"
    "s_addc_u32 s43, s43, 0\n"
    "global_load_lds_dwordx4 v4, s[42:43]\n"
    "s_add_u32 m0, m0, 0x400\n"
    "s_nop 0\n"
    "global_load_lds_dwordx4 v5, s[42:43]\n"
    "s_add_u32 s38, s38, 1\n"
    "s_add_u32 s44, s44, 1\n"
    "s_add_u32 s46, s44, 3\n"
    "s_and_b32 s46, s46, -4\n"
    "s_min_u32 s46, s46, 32\n"
    "s_cmp_ge_u32 s39, s46\n"
    "s_cbranch_scc1 .Lz_79\n"
    ".Lw_80:\n"
    "ds_read_b32 v1, v13 offset:68\n"
    "s_waitcnt lgkmcnt(0)\n"
    "v_readfirstlane_b32 s39, v1\n"
    "s_cmp_ge_u32 s39, s46\n"
    "s_cbranch_scc1 .Ld_81\n"
    "s_sleep 1\n"
    "s_add_u32 s63, s63, 1\n"
    "s_bitcmp1_b32 s63, 19\n"
    "s_cbranch_scc0 .Lw_80\n"
    "s_sub_u32 s63, s63, 1\n"
    ".Ld_81:\n"
    ".Lz_79:\n"
    "v_add_u32 v7, 1, v7\n"
    "s_waitcnt vmcnt(8)\n"
    "ds_write_b32 v9, v7 offset:16\n"
    ".Lw_82:\n"
    "ds_read_b32 v1, v13 offset:28\n"
    "s_waitcnt lgkmcnt(0)\n"
    "v_readfirstlane_b32 s60, v1\n"
    "s_cmp_ge_u32 s60, s37\n"
    "s_cbranch_scc1 .Ld_83\n"
    "s_sleep 1\n"
    "s_add_u32 s63, s63, 1\n"
    "s_bitcmp1_b32 s63, 19\n"
    "s_cbranch_scc0 .Lw_82\n"
    "s_sub_u32 s63, s63, 1\n"
    ".Ld_83:\n"
    "s_xor_b32 s45, s38, s59\n"
    "s_add_u32 s45, s45, s62\n"
    "s_and_b32 s45, s45, 31\n"
    "s_lshl_b32 s45, s45, 7\n"
    "s_add_u32 m0, s47, 139264\n"
    "s_add_u32 s42, s40, s45\n"
    "s_addc_u32 s43, s41, 0\n"
    "global_load_lds_dwordx4 v4, s[42:43]\n"
    "s_add_u32 m0, m0, 0x400\n"
    "s_nop 0\n"
    "global_load_lds_dwordx4 v5, s[42:43]\n"
    "s_add_u32 m0, m0, 0x400\n"
    "s_add_u32 s42, s42, 0x10000\n"
    "s_addc_u32 s43, s43, 0\n"
    "global_load_lds_dwordx4 v4, s[42:43]\n"
    "s_add_u32 m0, m0, 0x400\n"
    "s_nop 0\n"
    "global_load_lds_dwordx4 v5, s[42:43]\n"
    "s_add_u32 s38, s38, 1\n"
    "s_add_u32 s44, s44, 1\n"
    "s_add_u32 s46, s44, 3\n"
    "s_and_b32 s46, s46, -4\n"
    "s_min_u32 s46, s46, 32\n"
    "s_cmp_ge_u32 s39, s46\n"
    "s_cbranch_scc1 .Lz_84\n"
    ".Lw_85:\n"
    "ds_read_b32 v1, v13 offset:68\n"
    "s_waitcnt lgkmcnt(0)\n"
    "v_readfirstlane_b32 s39, v1\n"
    "s_cmp_ge_u32 s39, s46\n"
    "s_cbranch_scc1 .Ld_86\n"
    "s_sleep 1\n"
    "s_add_u32 s63, s63, 1\n"
    "s_bitcmp1_b32 s63, 19\n"
    "s_cbranch_scc0 .Lw_85\n"
    "s_sub_u32 s63, s63, 1\n"
    ".Ld_86:\n"
    ".Lz_84:\n"
    "v_add_u32 v7, 1, v7\n"
    "s_waitcnt vmcnt(8)\n"
    "ds_write_b32 v9, v7 offset:16\n"
    "s_cmp_eq_u32 s38, 32\n"
    "s_cbranch_scc1 .Lw_e\n"
    ".Lw_87:\n"
    "ds_read_b32 v1, v13 offset:32\n"
    "s_waitcnt lgkmcnt(0)\n"
    "v_readfirstlane_b32 s60, v1\n"
    "s_cmp_ge_u32 s60, s37\n"
    "s_cbranch_scc1 .Ld_88\n"
    "s_sleep 1\n"
    "s_add_u32 s63, s63, 1\n"
    "s_bitcmp1_b32 s63, 19\n"
    "s_cbranch_scc0 .Lw_87\n"
    "s_sub_u32 s63, s63, 1\n"
    ".Ld_88:\n"
    "s_xor_b32 s45, s38, s59\n"
    "s_add_u32 s45, s45, s62\n"
    "s_and_b32 s45, s45, 31\n"
    "s_lshl_b32 s45, s45, 7\n"
    "s_add_u32 m0, s47, 147456\n"
    "s_add_u32 s42, s40, s45\n"
    "s_addc_u32 s43, s41, 0\n"
    "global_load_lds_dwordx4 v4, s[42:43]\n"
    "s_add_u32 m0, m0, 0x400\n"
    "s_nop 0\n"
    "global_load_lds_dwordx4 v5, s[42:43]\n"
    "s_add_u32 m0, m0, 0x400\n"
    "s_add_u32 s42, s42, 0x10000\n"
    "s_addc_u32 s43, s43, 0\n"
    "global_load_lds_dwordx4 v4, s[42:43]\n"
    "s_add_u32 m0, m0, 0x400\n"
    "s_nop 0\n"
    "global_load_lds_dwordx4 v5, s[42:43]\n"
    "s_add_u32 s38, s38, 1\n"
    "s_add_u32 s44, s44, 1\n"
    "s_add_u32 s46, s44, 3\n"
    "s_and_b32 s46, s46, -4\n"
    "s_min_u32 s46, s46, 32\n"
    "s_cmp_ge_u32 s39, s46\n"
    "s_cbranch_scc1 .Lz_89\n"
    ".Lw_90:\n"
    "ds_read_b32 v1, v13 offset:68\n"
    "s_waitcnt lgkmcnt(0)\n"
    "v_readfirstlane_b32 s39, v1\n"
    "s_cmp_ge_u32 s39, s46\n"
    "s_cbranch_scc1 .Ld_91\n"
    "s_sleep 1\n"
    "s_add_u32 s63, s63, 1\n"
    "s_bitcmp1_b32 s63, 19\n"
    "s_cbranch_scc0 .Lw_90\n"
    "s_sub_u32 s63, s63, 1\n"
    ".Ld_91:\n"
    ".Lz_89:\n"
    "v_add_u32 v7, 1, v7\n"
    "s_waitcnt vmcnt(8)\n"
    "ds_write_b32 v9, v7 offset:16\n"
    ".Lw_92:\n"
    "ds_read_b32 v1, v13 offset:36\n"
    "s_waitcnt lgkmcnt(0)\n"
    "v_readfirstlane_b32 s60, v1\n"
    "s_cmp_ge_u32 s60, s37\n"
    "s_cbranch_scc1 .Ld_93\n"
    "s_sleep 1\n"
    "s_add_u32 s63, s63, 1\n"
    "s_bitcmp1_b32 s63, 19\n"
    "s_cbranch_scc0 .Lw_92\n"
    "s_sub_u32 s63, s63, 1\n"
    ".Ld_93:\n"
    "s_xor_b32 s45, s38, s59\n"
    "s_add_u32 s45, s45, s62\n"
    "s_and_b32 s45, s45, 31\n"
    "s_lshl_b32 s45, s45, 7\n"
    "s_add_u32 m0, s47, 0\n"
    "s_add_u32 s42, s40, s45\n"
    "s_addc_u32 s43, s41, 0\n"
    "global_load_lds_dwordx4 v4, s[42:43]\n"
    "s_add_u32 m0, m0, 0x400\n"
    "s_nop 0\n"
    "global_load_lds_dwordx4 v5, s[42:43]\n"
    "s_add_u32 m0, m0, 0x400\n"
    "s_add_u32 s42, s42, 0x10000\n"
    "s_addc_u32 s43, s43, 0\n"
    "global_load_lds_dwordx4 v4, s[42:43]\n"
    "s_add_u32 m0, m0, 0x400\n"
    "s_nop 0\n"
    "global_load_lds_dwordx4 v5, s[42:43]\n"
    "s_add_u32 s38, s38, 1\n"
    "s_add_u32 s44, s44, 1\n"
    "s_add_u32 s46, s44, 3\n"
    "s_and_b32 s46, s46, -4\n"
    "s_min_u32 s46, s46, 32\n"
    "s_cmp_ge_u32 s39, s46\n"
    "s_cbranch_scc1 .Lz_94\n"
    ".Lw_95:\n"
    "ds_read_b32 v1, v13 offset:68\n"
    "s_waitcnt lgkmcnt(0)\n"
    "v_readfirstlane_b32 s39, v1\n"
    "s_cmp_ge_u32 s39, s46\n"
    "s_cbranch_scc1 .Ld_96\n"
    "s_sleep 1\n"
    "s_add_u32 s63, s63, 1\n"
    "s_bitcmp1_b32 s63, 19\n"
    "s_cbranch_scc0 .Lw_95\n"
    "s_sub_u32 s63, s63, 1\n"
    ".Ld_96:\n"
    ".Lz_94:\n"
    "v_add_u32 v7, 1, v7\n"
    "s_waitcnt vmcnt(8)\n"
    "ds_write_b32 v9, v7 offset:16\n"
    ".Lw_97:\n"
    "ds_read_b32 v1, v13 offset:40\n"
    "s_waitcnt lgkmcnt(0)\n"
    "v_readfirstlane_b32 s60, v1\n"
    "s_cmp_ge_u32 s60, s37\n"
    "s_cbranch_scc1 .Ld_98\n"
    "s_sleep 1\n"
    "s_add_u32 s63, s63, 1\n"
    "s_bitcmp1_b32 s63, 19\n"
    "s_cbranch_scc0 .Lw_97\n"
    "s_sub_u32 s63, s63, 1\n"
    ".Ld_98:\n"
    "s_xor_b32 s45, s38, s59\n"
    "s_add_u32 s45, s45, s62\n"
    "s_and_b32 s45, s45, 31\n"
    "s_lshl_b32 s45, s45, 7\n"
    "s_add_u32 m0, s47, 8192\n"
    "s_add_u32 s42, s40, s45\n"
    "s_addc_u32 s43, s41, 0\n"
    "global_load_lds_dwordx4 v4, s[42:43]\n"
    "s_add_u32 m0, m0, 0x400\n"
    "s_nop 0\n"
    "global_load_lds_dwordx4 v5, s[42:43]\n"
    "s_add_u32 m0, m0, 0x400\n"
    "s_add_u32 s42, s42, 0x10000\n"
    "s_addc_u32 s43, s43, 0\n"
    "global_load_lds_dwordx4 v4, s[42:43]\n"
    "s_add_u32 m0, m0, 0x400\n"
    "s_nop 0\n"
    "global_load_lds_dwordx4 v5, s[42:43]\n"
    "s_add_u32 s38, s38, 1\n"
    "s_add_u32 s44, s44, 1\n"
    "s_add_u32 s46, s44, 3\n"
    "s_and_b32 s46, s46, -4\n"
    "s_min_u32 s46, s46, 32\n"
    "s_cmp_ge_u32 s39, s46\n"
    "s_cbranch_scc1 .Lz_99\n"
    ".Lw_100:\n"
    "ds_read_b32 v1, v13 offset:68\n"
    "s_waitcnt lgkmcnt(0)\n"
    "v_readfirstlane_b32 s39, v1\n"
    "s_cmp_ge_u32 s39, s46\n"
    "s_cbranch_scc1 .Ld_101\n"
    "s_sleep 1\n"
    "s_add_u32 s63, s63, 1\n"
    "s_bitcmp1_b32 s63, 19\n"
    "s_cbranch_scc0 .Lw_100\n"
    "s_sub_u32 s63, s63, 1\n"
    ".Ld_101:\n"
    ".Lz_99:\n"
    "v_add_u32 v7, 1, v7\n"
    "s_waitcnt vmcnt(8)\n"
    "ds_write_b32 v9, v7 offset:16\n"
    ".Lw_102:\n"
    "ds_read_b32 v1, v13 offset:44\n"
    "s_waitcnt lgkmcnt(0)\n"
    "v_readfirstlane_b32 s60, v1\n"
    "s_cmp_ge_u32 s60, s37\n"
    "s_cbranch_scc1 .Ld_103\n"
    "s_sleep 1\n"
    "s_add_u32 s63, s63, 1\n"
    "s_bitcmp1_b32 s63, 19\n"
    "s_cbranch_scc0 .Lw_102\n"
    "s_sub_u32 s63, s63, 1\n"
    ".Ld_103:\n"
    "s_xor_b32 s45, s38, s59\n"
    "s_add_u32 s45, s45, s62\n"
    "s_and_b32 s45, s45, 31\n"
    "s_lshl_b32 s45, s45, 7\n"
    "s_add_u32 m0, s47, 16384\n"
    "s_add_u32 s42, s40, s45\n"
    "s_addc_u32 s43, s41, 0\n"
    "global_load_lds_dwordx4 v4, s[42:43]\n"
    "s_add_u32 m0, m0, 0x400\n"
    "s_nop 0\n"
    "global_load_lds_dwordx4 v5, s[42:43]\n"
    "s_add_u32 m0, m0, 0x400\n"
    "s_add_u32 s42, s42, 0x10000\n"
    "s_addc_u32 s43, s43, 0\n"
    "global_load_lds_dwordx4 v4, s[42:43]\n"
    "s_add_u32 m0, m0, 0x400\n"
    "s_nop 0\n"
    "global_load_lds_dwordx4 v5, s[42:43]\n"
    "s_add_u32 s38, s38, 1\n"
    "s_add_u32 s44, s44, 1\n"
    "s_add_u32 s46, s44, 3\n"
    "s_and_b32 s46, s46, -4\n"
    "s_min_u32 s46, s46, 32\n"
    "s_cmp_ge_u32 s39, s46\n"
    "s_cbranch_scc1 .Lz_104\n"
    ".Lw_105:\n"
    "ds_read_b32 v1, v13 offset:68\n"
    "s_waitcnt lgkmcnt(0)\n"
    "v_readfirstlane_b32 s39, v1\n"
    "s_cmp_ge_u32 s39, s46\n"
    "s_cbranch_scc1 .Ld_106\n"
    "s_sleep 1\n"
    "s_add_u32 s63, s63, 1\n"
    "s_bitcmp1_b32 s63, 19\n"
    "s_cbranch_scc0 .Lw_105\n"
    "s_sub_u32 s63, s63, 1\n"
    ".Ld_106:\n"
    ".Lz_104:\n"
    "v_add_u32 v7, 1, v7\n"
    "s_waitcnt vmcnt(8)\n"
    "ds_write_b32 v9, v7 offset:16\n"
    "s_add_u32 s37, s37, 4\n"
    "s_branch .Lw_l\n"
    ".Lw_e:\n"
    "s_add_u32 s44, s44, 1\n"
    "s_add_u32 s46, s44, 3\n"
    "s_and_b32 s46, s46, -4\n"
    "s_min_u32 s46, s46, 32\n"
    "s_cmp_ge_u32 s39, s46\n"
    "s_cbranch_scc1 .Lz_107\n"
    ".Lw_108:\n"
    "ds_read_b32 v1, v13 offset:68\n"
    "s_waitcnt lgkmcnt(0)\n"
    "v_readfirstlane_b32 s39, v1\n"
    "s_cmp_ge_u32 s39, s46\n"
    "s_cbranch_scc1 .Ld_109\n"
    "s_sleep 1\n"
    "s_add_u32 s63, s63, 1\n"
    "s_bitcmp1_b32 s63, 19\n"
    "s_cbranch_scc0 .Lw_108\n"
    "s_sub_u32 s63, s63, 1\n"
    ".Ld_109:\n"
    ".Lz_107:\n"
    "v_add_u32 v7, 1, v7\n"
    "s_waitcnt vmcnt(4)\n"
    "ds_write_b32 v9, v7 offset:16\n"
    "s_add_u32 s44, s44, 1\n"
    "s_add_u32 s46, s44, 3\n"
    "s_and_b32 s46, s46, -4\n"
    "s_min_u32 s46, s46, 32\n"
    "s_cmp_ge_u32 s39, s46\n"
    "s_cbranch_scc1 .Lz_110\n"
    ".Lw_111:\n"
    "ds_read_b32 v1, v13 offset:68\n"
    "s_waitcnt lgkmcnt(0)\n"
    "v_readfirstlane_b32 s39, v1\n"
    "s_cmp_ge_u32 s39, s46\n"
    "s_cbranch_scc1 .Ld_112\n"
    "s_sleep 1\n"
    "s_add_u32 s63, s63, 1\n"
    "s_bitcmp1_b32 s63, 19\n"
    "s_cbranch_scc0 .Lw_111\n"
    "s_sub_u32 s63, s63, 1\n"
    ".Ld_112:\n"
    ".Lz_110:\n"
    "v_add_u32 v7, 1, v7\n"
    "s_waitcnt vmcnt(0)\n"
    "ds_write_b32 v9, v7 offset:16\n"
    ".Lquit:\n"
    "s_cmp_eq_u32 s24, 13\n"
    "s_cbranch_scc1 .Lwc\n"
    "s_branch .Lq_o\n"
    ".Lgw:\n"
    "s_sub_u32 s55, s24, 8\n"
    "v_mov_b32 v119, 157696\n"
    "s_cmp_lt_u32 s26, 28\n"
    "s_cbranch_scc0 .Lfin0\n"
    "s_lshl2_add_u32 s57, s30, s55\n"
    "s_lshl2_add_u32 s56, s28, s55\n"
    "v_and_b32 v8, 31, v0\n"
    "v_lshrrev_b32 v9, 5, v0\n"
    "v_and_b32 v4, 0x30, v0\n"
    "v_lshlrev_b32 v4, 6, v4\n"
    "v_and_b32 v1, 12, v0\n"
    "v_lshl_or_b32 v4, v1, 5, v4\n"
    "v_and_b32 v1, 24, v0\n"
    "v_lshl_or_b32 v4, v1, 2, v4\n"
    "v_and_b32 v1, 3, v0\n"
    "v_lshl_or_b32 v4, v1, 3, v4\n"
    "s_lshl_b32 s40, s55, 12\n"
    "s_add_u32 s40, s40, 65536\n"
    "v_add_u32 v4, s40, v4\n"
    "v_xor_b32 v5, 32, v4\n"
    "v_xor_b32 v6, 64, v4\n"
    "v_xor_b32 v7, 96, v4\n"
    "s_lshr_b32 s40, s55, 1\n"
    "s_lshl_b32 s40, s40, 2\n"
    "v_add_u32 v2, s40, v13\n"
    "v_mul_u32_u24 v10, 229376, v9\n"
    "v_lshl_add_u32 v10, v8, 4, v10\n"
    "s_lshl_b32 s40, s57, 9\n"
    "s_add_u32 s40, s40, 23134208\n"
    "s_add_u32 s46, s10, s40\n"
    "s_addc_u32 s47, s11, 0\n"
    "s_lshl_b32 s40, s55, 8\n"
    "s_add_u32 s40, s40, 157952\n"
    "v_lshl_add_u32 v11, v8, 3, s40\n"
    "v_lshrrev_b32 v15, 4, v0\n"
    "v_lshl_add_u32 v12, v15, 6, s40\n"
    "v_and_b32 v14, 3, v0\n"
    "v_lshlrev_b32 v14, 4, v14\n"
    "v_and_b32 v1, 4, v0\n"
    "v_lshl_or_b32 v14, v1, 8, v14\n"
    "v_and_b32 v1, 8, v0\n"
    "v_lshl_or_b32 v14, v1, 3, v14\n"
    "v_lshl_or_b32 v14, v15, 8, v14\n"
    "s_lshl_b32 s40, s31, 7\n"
    "s_add_u32 s40, s40, s57\n"
    "s_lshl_b32 s40, s40, 11\n"
    "s_add_u32 s40, s40, 6291456\n"
    "s_add_u32 s48, s10, s40\n"
    "s_addc_u32 s49, s11, 0\n"
    "s_add_u32 s50, s48, 131072\n"
    "s_addc_u32 s51, s49, 0\n"
    "s_sleep 40\n"
    ".Lw_113:\n"
    "ds_read_b32 v1, v2 offset:56\n"
    "s_waitcnt lgkmcnt(0)\n"
    "v_readfirstlane_b32 s60, v1\n"
    "s_cmp_ge_u32 s60, 1\n"
    "s_cbranch_scc1 .Ld_114\n"
    "s_sleep 1\n"
    "s_add_u32 s63, s63, 1\n"
    "s_bitcmp1_b32 s63, 19\n"
    "s_cbranch_scc0 .Lw_113\n"
    "s_sub_u32 s63, s63, 1\n"
    ".Ld_114:\n"
    "ds_read_b64_tr_b16 v[32:33], v4\n"
    "ds_read_b64_tr_b16 v[34:35], v4 offset:512\n"
    "ds_read_b64_tr_b16 v[36:37], v5\n"
    "ds_read_b64_tr_b16 v[38:39], v5 offset:512\n"
    "ds_read_b64_tr_b16 v[40:41], v6\n"
    "ds_read_b64_tr_b16 v[42:43], v6 offset:512\n"
    "ds_read_b64_tr_b16 v[44:45], v7\n"
    "ds_read_b64_tr_b16 v[46:47], v7 offset:512\n"
    "s_waitcnt lgkmcnt(0)\n"
    "s_mov_b64 exec, 1\n"
    "v_mov_b32 v1, 1\n"
    "ds_add_u32 v13, v1 offset:64\n"
    "s_mov_b64 exec, -1\n"
    "v_lshlrev_b32 v80, 16, v32\n"
    "v_lshlrev_b32 v82, 16, v33\n"
    "v_lshlrev_b32 v84, 16, v34\n"
    "v_lshlrev_b32 v86, 16, v35\n"
    "v_lshlrev_b32 v88, 16, v36\n"
    "v_lshlrev_b32 v90, 16, v37\n"
    "v_lshlrev_b32 v92, 16, v38\n"
    "v_lshlrev_b32 v94, 16, v39\n"
    "v_lshlrev_b32 v96, 16, v40\n"
    "v_lshlrev_b32 v98, 16, v41\n"
    "v_lshlrev_b32 v100, 16, v42\n"
    "v_lshlrev_b32 v102, 16, v43\n"
    "v_lshlrev_b32 v104, 16, v44\n"
    "v_lshlrev_b32 v106, 16, v45\n"
    "v_lshlrev_b32 v108, 16, v46\n"
    "v_lshlrev_b32 v110, 16, v47\n"
    "v_and_b32 v81, 0xffff0000, v32\n"
    "v_and_b32 v83, 0xffff0000, v33\n"
    "v_and_b32 v85, 0xffff0000, v34\n"
    "v_and_b32 v87, 0xffff0000, v35\n"
    "v_and_b32 v89, 0xffff0000, v36\n"
    "v_and_b32 v91, 0xffff0000, v37\n"
    "v_and_b32 v93, 0xffff0000, v38\n"
    "v_and_b32 v95, 0xffff0000, v39\n"
    "v_and_b32 v97, 0xffff0000, v40\n"
    "v_and_b32 v99, 0xffff0000, v41\n"
    "v_and_b32 v101, 0xffff0000, v42\n"
    "v_and_b32 v103, 0xffff0000, v43\n"
    "v_and_b32 v105, 0xffff0000, v44\n"
    "v_and_b32 v107, 0xffff0000, v45\n"
    "v_and_b32 v109, 0xffff0000, v46\n"
    "v_and_b32 v111, 0xffff0000, v47\n"
    ".Lw_115:\n"
    "ds_read_b32 v1, v13 offset:96\n"
    "s_waitcnt lgkmcnt(0)\n"
    "v_readfirstlane_b32 s60, v1\n"
    "s_cmp_ge_u32 s60, 1\n"
    "s_cbranch_scc1 .Ld_116\n"
    "s_sleep 1\n"
    "s_add_u32 s63, s63, 1\n"
    "s_bitcmp1_b32 s63, 19\n"
    "s_cbranch_scc0 .Lw_115\n"
    "s_sub_u32 s63, s63, 1\n"
    ".Ld_116:\n"
    "s_mov_b32 s36, 0\n"
    ".Lz_l:\n"
    "s_mov_b64 s[42:43], s[46:47]\n"
    "global_load_dwordx4 v[48:51], v10, s[42:43] sc1\n"
    "s_add_u32 s42, s42, 0x8000\n"
    "s_addc_u32 s43, s43, 0\n"
    "global_load_dwordx4 v[52:55], v10, s[42:43] sc1\n"
    "s_add_u32 s42, s42, 0x8000\n"
    "s_addc_u32 s43, s43, 0\n"
    "global_load_dwordx4 v[56:59], v10, s[42:43] sc1\n"
    "s_add_u32 s42, s42, 0x8000\n"
    "s_addc_u32 s43, s43, 0\n"
    "global_load_dwordx4 v[60:63], v10, s[42:43] sc1\n"
    "s_add_u32 s42, s42, 0x8000\n"
    "s_addc_u32 s43, s43, 0\n"
    "global_load_dwordx4 v[64:67], v10, s[42:43] sc1\n"
    "s_add_u32 s42, s42, 0x8000\n"
    "s_addc_u32 s43, s43, 0\n"
    "global_load_dwordx4 v[68:71], v10, s[42:43] sc1\n"
    "s_add_u32 s42, s42, 0x8000\n"
    "s_addc_u32 s43, s43, 0\n"
    "global_load_dwordx4 v[72:75], v10, s[42:43] sc1\n"
    "s_add_u32 s36, s36, 1\n"
    "s_waitcnt vmcnt(0)\n"
    "v_xor_b32 v49, s34, v49\n"
    "v_xor_b32 v53, s34, v53\n"
    "v_xor_b32 v57, s34, v57\n"
    "v_xor_b32 v61, s34, v61\n"
    "v_xor_b32 v65, s34, v65\n"
    "v_xor_b32 v69, s34, v69\n"
    "v_xor_b32 v73, s34, v73\n"
    "v_xor_b32 v51, s35, v51\n"
    "v_xor_b32 v55, s35, v55\n"
    "v_xor_b32 v59, s35, v59\n"
    "v_xor_b32 v63, s35, v63\n"
    "v_xor_b32 v67, s35, v67\n"
    "v_xor_b32 v71, s35, v71\n"
    "v_xor_b32 v75, s35, v75\n"
    "v_or_b32 v1, v49, v51\n"
    "v_or3_b32 v1, v1, v53, v55\n"
    "v_or3_b32 v1, v1, v57, v59\n"
    "v_or3_b32 v1, v1, v61, v63\n"
    "v_or3_b32 v1, v1, v65, v67\n"
    "v_or3_b32 v1, v1, v69, v71\n"
    "v_or3_b32 v1, v1, v73, v75\n"
    "v_cmp_ne_u32 vcc, 0, v1\n"
    "s_cmp_eq_u64 vcc, 0\n"
    "s_cbranch_scc1 .Lz_d\n"
    "s_sleep 2\n"
    "s_cmp_lt_u32 s36, 32768\n"
    "s_cbranch_scc1 .Lz_l\n"
    ".Lz_d:\n"
    "v_add_f32 v48, v48, v52\n"
    "v_add_f32 v50, v50, v54\n"
    "v_add_f32 v48, v48, v56\n"
    "v_add_f32 v50, v50, v58\n"
    "v_add_f32 v48, v48, v60\n"
    "v_add_f32 v50, v50, v62\n"
    "v_add_f32 v48, v48, v64\n"
    "v_add_f32 v50, v50, v66\n"
    "v_add_f32 v48, v48, v68\n"
    "v_add_f32 v50, v50, v70\n"
    "v_add_f32 v48, v48, v72\n"
    "v_add_f32 v50, v50, v74\n"
    "v_mov_b32 v49, v48\n"
    "v_mov_b32 v51, v50\n"
    "s_nop 1\n"
    "v_permlane32_swap_b32 v48, v49\n"
    "v_permlane32_swap_b32 v50, v51\n"
    "v_add_f32 v2, v48, v49\n"
    "v_add_f32 v3, v50, v51\n"
    "v_mul_f32 v2, 0x3a924925, v2\n"
    "v_mul_f32 v3, 0x3a924925, v3\n"
    "v_fma_f32 v3, -v2, v2, v3\n"
    "v_max_f32 v3, 0, v3\n"
    "v_cvt_pk_bf16_f32 v3, v3, v3\n"
    "v_lshlrev_b32 v3, 16, v3\n"
    "v_add_f32 v3, 0x3727c5ac, v3\n"
    "v_cvt_pk_bf16_f32 v3, v3, v3\n"
    "v_lshlrev_b32 v3, 16, v3\n"
    "v_sqrt_f32 v3, v3\n"
    "v_cvt_pk_bf16_f32 v2, v2, v2\n"
    "v_lshlrev_b32 v2, 16, v2\n"
    "v_cvt_pk_bf16_f32 v3, v3, v3\n"
    "v_lshlrev_b32 v3, 16, v3\n"
    "v_rcp_f32 v3, v3\n"
    "v_xor_b32 v2, 0x80000000, v2\n"
    "v_mul_f32 v2, v2, v3\n"
    "ds_write_b64 v11, v[2:3]\n"
    "ds_read_b128 v[48:51], v12 offset:0\n"
    "ds_read_b128 v[52:55], v12 offset:16\n"
    "ds_read_b128 v[56:59], v12 offset:32\n"
    "ds_read_b128 v[60:63], v12 offset:48\n"
    "s_waitcnt lgkmcnt(0)\n"
    "v_fma_f32 v80, v80, v49, v48\n"
    "v_fma_f32 v81, v81, v51, v50\n"
    "v_fma_f32 v82, v82, v53, v52\n"
    "v_fma_f32 v83, v83, v55, v54\n"
    "v_fma_f32 v84, v84, v57, v56\n"
    "v_fma_f32 v85, v85, v59, v58\n"
    "v_fma_f32 v86, v86, v61, v60\n"
    "v_fma_f32 v87, v87, v63, v62\n"
    "v_cvt_pk_bf16_f32 v32, v80, v81\n"
    "v_cvt_pk_bf16_f32 v33, v82, v83\n"
    "v_cvt_pk_bf16_f32 v34, v84, v85\n"
    "v_cvt_pk_bf16_f32 v35, v86, v87\n"
    "global_store_dwordx4 v14, v[32:35], s[48:49] offset:0 sc1\n"
    "v_fma_f32 v88, v88, v49, v48\n"
    "v_fma_f32 v89, v89, v51, v50\n"
    "v_fma_f32 v90, v90, v53, v52\n"
    "v_fma_f32 v91, v91, v55, v54\n"
    "v_fma_f32 v92, v92, v57, v56\n"
    "v_fma_f32 v93, v93, v59, v58\n"
    "v_fma_f32 v94, v94, v61, v60\n"
    "v_fma_f32 v95, v95, v63, v62\n"
    "v_cvt_pk_bf16_f32 v36, v88, v89\n"
    "v_cvt_pk_bf16_f32 v37, v90, v91\n"
    "v_cvt_pk_bf16_f32 v38, v92, v93\n"
    "v_cvt_pk_bf16_f32 v39, v94, v95\n"
    "global_store_dwordx4 v14, v[36:39], s[48:49] offset:128 sc1\n"
    "v_fma_f32 v96, v96, v49, v48\n"
    "v_fma_f32 v97, v97, v51, v50\n"
    "v_fma_f32 v98, v98, v53, v52\n"
    "v_fma_f32 v99, v99, v55, v54\n"
    "v_fma_f32 v100, v100, v57, v56\n"
    "v_fma_f32 v101, v101, v59, v58\n"
    "v_fma_f32 v102, v102, v61, v60\n"
    "v_fma_f32 v103, v103, v63, v62\n"
    "v_cvt_pk_bf16_f32 v40, v96, v97\n"
    "v_cvt_pk_bf16_f32 v41, v98, v99\n"
    "v_cvt_pk_bf16_f32 v42, v100, v101\n"
    "v_cvt_pk_bf16_f32 v43, v102, v103\n"
    "global_store_dwordx4 v14, v[40:43], s[50:51] offset:0 sc1\n"
    "v_fma_f32 v104, v104, v49, v48\n"
    "v_fma_f32 v105, v105, v51, v50\n"
    "v_fma_f32 v106, v106, v53, v52\n"
    "v_fma_f32 v107, v107, v55, v54\n"
    "v_fma_f32 v108, v108, v57, v56\n"
    "v_fma_f32 v109, v109, v59, v58\n"
    "v_fma_f32 v110, v110, v61, v60\n"
    "v_fma_f32 v111, v111, v63, v62\n"
    "v_cvt_pk_bf16_f32 v44, v104, v105\n"
    "v_cvt_pk_bf16_f32 v45, v106, v107\n"
    "v_cvt_pk_bf16_f32 v46, v108, v109\n"
    "v_cvt_pk_bf16_f32 v47, v110, v111\n"
    "global_store_dwordx4 v14, v[44:47], s[50:51] offset:128 sc1\n"
    "s_lshl_b32 s41, s31, 6\n"
    "s_add_u32 s41, s41, s57\n"
    "s_lshl_b32 s41, s41, 7\n"
    "s_add_u32 s41, s41, 23719936\n"
    "v_mov_b32 v2, s34\n"
    "v_mov_b32 v3, s35\n"
    "v_lshl_add_u32 v4, v0, 3, s41\n"
    "s_waitcnt vmcnt(0)\n"
    "s_mov_b64 exec, 0xffff\n"
    "global_store_dwordx2 v4, v[2:3], s[10:11] sc1\n"
    "s_mov_b64 exec, -1\n"
    "s_waitcnt lgkmcnt(0)\n"
    "s_mov_b64 exec, 1\n"
    "v_mov_b32 v1, 1\n"
    "ds_add_u32 v13, v1 offset:100\n"
    "s_mov_b64 exec, -1\n"
    "v_and_b32 v8, 15, v0\n"
    "v_mov_b32 v120, -1\n"
    "v_mov_b32 v121, -1\n"
    "v_mov_b32 v122, -1\n"
    "v_mov_b32 v123, -1\n"
    "s_lshl_b32 s41, s52, 5\n"
    "s_add_u32 s41, s41, s56\n"
    "s_lshl_b32 s41, s41, 7\n"
    "s_add_u32 s41, s41, 23834624\n"
    "s_mov_b32 s57, s41\n"
    "s_lshl2_add_u32 s41, s55, 157904\n"
    "v_mov_b32 v125, s41\n"
    "v_lshrrev_b32 v1, 1, v8\n"
    "v_xor_b32 v1, v1, v15\n"
    "v_lshlrev_b32 v1, 4, v1\n"
    "v_lshl_or_b32 v1, v8, 7, v1\n"
    "v_add_u32 v112, 131072, v1\n"
    "v_xor_b32 v113, 64, v112\n"
    "v_mov_b32 v126, v1\n"
    "v_xor_b32 v127, 64, v1\n"
    "v_lshlrev_b32 v116, 4, v0\n"
    "v_mov_b32 v124, v116\n"
    "v_mov_b32 v118, 1\n"
    "s_lshl_b32 s40, s56, 17\n"
    "s_add_u32 s40, s40, 6291456\n"
    "s_add_u32 s46, s10, s40\n"
    "s_addc_u32 s47, s11, 0\n"
    "s_mul_i32 s40, s52, 28\n"
    "s_add_u32 s40, s40, s56\n"
    "s_lshl_b32 s40, s40, 12\n"
    "s_add_u32 s41, s40, 18874368\n"
    "s_add_u32 s40, s40, 18874368\n"
    "s_add_u32 s48, s10, s40\n"
    "s_addc_u32 s49, s11, 0\n"
    "s_add_u32 s50, s10, s41\n"
    "s_addc_u32 s51, s11, 0\n"
    "s_lshl_b32 s62, s25, 2\n"
    "s_lshl_b32 s59, s27, 1\n"
    "s_xor_b32 s59, s59, s28\n"
    "s_lshl_b32 s59, s59, 1\n"
    "s_mov_b32 s39, -1\n"
    "s_mov_b32 s36, 0\n"
    "v_mov_b64 v[0:1], 0\n"
    "v_mov_b64 v[2:3], 0\n"
    "v_mov_b64 v[4:5], 0\n"
    "v_mov_b64 v[6:7], 0\n"
    "v_mov_b64 v[8:9], 0\n"
    "v_mov_b64 v[10:11], 0\n"
    "v_mov_b64 v[12:13], 0\n"
    "v_mov_b64 v[14:15], 0\n"
    "v_mov_b64 v[16:17], 0\n"
    "v_mov_b64 v[18:19], 0\n"
    "v_mov_b64 v[20:21], 0\n"
    "v_mov_b64 v[22:23], 0\n"
    "v_mov_b64 v[24:25], 0\n"
    "v_mov_b64 v[26:27], 0\n"
    "v_mov_b64 v[28:29], 0\n"
    "v_mov_b64 v[30:31], 0\n"
    "v_bfe_u32 v32, v124, 4, 4\n"
    "s_lshl_b32 s40, s52, 7\n"
    "v_lshl_add_u32 v32, v32, 1, s40\n"
    "v_mov_b32 v120, 0\n"
    "v_mov_b32 v121, 0\n"
    "v_mov_b32 v122, 0\n"
    "v_mov_b32 v123, 0\n"
    "global_load_short_d16_hi v120, v32, s[18:19] offset:0\n"
    "global_load_short_d16_hi v121, v32, s[18:19] offset:32\n"
    "global_load_short_d16_hi v122, v32, s[18:19] offset:64\n"
    "global_load_short_d16_hi v123, v32, s[18:19] offset:96\n"
    "s_add_u32 s39, s39, 1\n"
    "s_xor_b32 s40, s39, s59\n"
    "s_add_u32 s40, s40, s62\n"
    "s_and_b32 s40, s40, 31\n"
    "s_lshl_b32 s40, s40, 12\n"
    "s_add_u32 s42, s46, s40\n"
    "s_addc_u32 s43, s47, 0\n"
    "s_mov_b32 s37, 1\n"
    ".Lw_117:\n"
    "ds_read_b64 v[114:115], v119 offset:16\n"
    "s_waitcnt lgkmcnt(0)\n"
    "v_min_u32 v117, v114, v115\n"
    "s_nop 0\n"
    "v_readfirstlane_b32 s60, v117\n"
    "s_cmp_ge_u32 s60, 1\n"
    "s_cbranch_scc1 .Ld_118\n"
    "s_sleep 1\n"
    "s_add_u32 s63, s63, 1\n"
    "s_bitcmp1_b32 s63, 19\n"
    "s_cbranch_scc0 .Lw_117\n"
    "s_sub_u32 s63, s63, 1\n"
    ".Ld_118:\n"
    "global_load_dwordx4 v[32:35], v116, s[42:43] offset:0 sc1\n"
    "global_load_dwordx4 v[36:39], v116, s[42:43] offset:1024 sc1\n"
    "global_load_dwordx4 v[40:43], v116, s[42:43] offset:2048 sc1\n"
    "global_load_dwordx4 v[44:47], v116, s[42:43] offset:3072 sc1\n"
    "s_add_u32 s39, s39, 1\n"
    "s_xor_b32 s40, s39, s59\n"
    "s_add_u32 s40, s40, s62\n"
    "s_and_b32 s40, s40, 31\n"
    "s_lshl_b32 s40, s40, 12\n"
    "s_add_u32 s42, s46, s40\n"
    "s_addc_u32 s43, s47, 0\n"
    "global_load_dwordx4 v[48:51], v116, s[42:43] offset:0 sc1\n"
    "global_load_dwordx4 v[52:55], v116, s[42:43] offset:1024 sc1\n"
    "global_load_dwordx4 v[56:59], v116, s[42:43] offset:2048 sc1\n"
    "global_load_dwordx4 v[60:63], v116, s[42:43] offset:3072 sc1\n"
    "s_add_u32 s39, s39, 1\n"
    "s_xor_b32 s40, s39, s59\n"
    "s_add_u32 s40, s40, s62\n"
    "s_and_b32 s40, s40, 31\n"
    "s_lshl_b32 s40, s40, 12\n"
    "s_add_u32 s42, s46, s40\n"
    "s_addc_u32 s43, s47, 0\n"
    "global_load_dwordx4 v[64:67], v116, s[42:43] offset:0 sc1\n"
    "global_load_dwordx4 v[68:71], v116, s[42:43] offset:1024 sc1\n"
    "ds_read_b128 v[80:83], v112 offset:0\n"
    "ds_read_b128 v[84:87], v112 offset:2048\n"
    "ds_read_b128 v[88:91], v112 offset:4096\n"
    "ds_read_b128 v[92:95], v112 offset:6144\n"
    "ds_read_b128 v[96:99], v113 offset:0\n"
    "ds_read_b128 v[100:103], v113 offset:2048\n"
    "s_setprio 2\n"
    ".Lgl:\n"
    "s_waitcnt vmcnt(8) lgkmcnt(4)\n"
    "v_mfma_f32_16x16x32_bf16 v[0:3], v[32:35], v[80:83], v[0:3]\n"
    "ds_read_b128 v[104:107], v113 offset:4096\n"
    "ds_read_b128 v[108:111], v113 offset:6144\n"
    "v_mfma_f32_16x16x32_bf16 v[4:7], v[36:39], v[80:83], v[4:7]\n"
    "s_mov_b64 exec, 1\n"
    "ds_add_u32 v119, v118 offset:24\n"
    "s_mov_b64 exec, -1\n"
    "ds_read_b64 v[114:115], v119 offset:16\n"
    "v_mfma_f32_16x16x32_bf16 v[8:11], v[32:35], v[84:87], v[8:11]\n"
    "ds_read_b32 v117, v125\n"
    "global_load_dwordx4 v[72:75], v116, s[42:43] offset:2048 sc1\n"
    "v_mfma_f32_16x16x32_bf16 v[12:15], v[36:39], v[84:87], v[12:15]\n"
    "global_load_dwordx4 v[76:79], v116, s[42:43] offset:3072 sc1\n"
    "s_waitcnt lgkmcnt(7)\n"
    "v_mfma_f32_16x16x32_bf16 v[16:19], v[32:35], v[88:91], v[16:19]\n"
    "s_add_u32 s39, s39, 1\n"
    "s_xor_b32 s40, s39, s59\n"
    "s_add_u32 s40, s40, s62\n"
    "v_mfma_f32_16x16x32_bf16 v[20:23], v[36:39], v[88:91], v[20:23]\n"
    "s_and_b32 s40, s40, 31\n"
    "s_lshl_b32 s40, s40, 12\n"
    "s_add_u32 s42, s46, s40\n"
    "v_mfma_f32_16x16x32_bf16 v[24:27], v[32:35], v[92:95], v[24:27]\n"
    "s_addc_u32 s43, s47, 0\n"
    "s_cmp_eq_u32 s39, 33\n"
    "s_cselect_b32 s40, 0, -1\n"
    "v_mfma_f32_16x16x32_bf16 v[28:31], v[36:39], v[92:95], v[28:31]\n"
    "v_and_b32 v116, s40, v116\n"
    "s_add_u32 s37, s37, 1\n"
    "s_min_u32 s37, s37, 32\n"
    "s_waitcnt vmcnt(8) lgkmcnt(0)\n"
    "v_readfirstlane_b32 s38, v114\n"
    "v_readfirstlane_b32 s41, v115\n"
    "v_readfirstlane_b32 s44, v117\n"
    "v_mfma_f32_16x16x32_bf16 v[0:3], v[40:43], v[96:99], v[0:3]\n"
    "s_min_u32 s38, s38, s41\n"
    "s_cmp_ge_u32 s38, s37\n"
    "v_mfma_f32_16x16x32_bf16 v[4:7], v[44:47], v[96:99], v[4:7]\n"
    "s_cbranch_scc0 .Lgs0\n"
    ".Lgr0:\n"
    "v_mfma_f32_16x16x32_bf16 v[8:11], v[40:43], v[100:103], v[8:11]\n"
    "ds_read_b128 v[80:83], v112 offset:8192\n"
    "ds_read_b128 v[84:87], v112 offset:10240\n"
    "v_mfma_f32_16x16x32_bf16 v[12:15], v[44:47], v[100:103], v[12:15]\n"
    "ds_read_b128 v[88:91], v112 offset:12288\n"
    "ds_read_b128 v[92:95], v112 offset:14336\n"
    "v_mfma_f32_16x16x32_bf16 v[16:19], v[40:43], v[104:107], v[16:19]\n"
    "ds_read_b128 v[96:99], v113 offset:8192\n"
    "v_mfma_f32_16x16x32_bf16 v[20:23], v[44:47], v[104:107], v[20:23]\n"
    "ds_read_b128 v[100:103], v113 offset:10240\n"
    "v_mfma_f32_16x16x32_bf16 v[24:27], v[40:43], v[108:111], v[24:27]\n"
    "global_load_dwordx4 v[32:35], v116, s[42:43] offset:0 sc1\n"
    "v_mfma_f32_16x16x32_bf16 v[28:31], v[44:47], v[108:111], v[28:31]\n"
    "global_load_dwordx4 v[36:39], v116, s[42:43] offset:1024 sc1\n"
    "s_mul_i32 s45, s39, 2460\n"
    "s_add_i32 s45, s45, 26540\n"
    "s_setprio 2\n"
    "s_cmp_gt_i32 s44, s45\n"
    "s_cbranch_scc0 .Lgc0\n"
    "s_setprio 3\n"
    ".Lgc0:\n"
    "s_waitcnt vmcnt(8) lgkmcnt(4)\n"
    "v_mfma_f32_16x16x32_bf16 v[0:3], v[48:51], v[80:83], v[0:3]\n"
    "ds_read_b128 v[104:107], v113 offset:12288\n"
    "ds_read_b128 v[108:111], v113 offset:14336\n"
    "v_mfma_f32_16x16x32_bf16 v[4:7], v[52:55], v[80:83], v[4:7]\n"
    "s_mov_b64 exec, 1\n"
    "ds_add_u32 v119, v118 offset:28\n"
    "s_mov_b64 exec, -1\n"
    "ds_read_b64 v[114:115], v119 offset:16\n"
    "v_mfma_f32_16x16x32_bf16 v[8:11], v[48:51], v[84:87], v[8:11]\n"
    "global_load_dwordx4 v[40:43], v116, s[42:43] offset:2048 sc1\n"
    "v_mfma_f32_16x16x32_bf16 v[12:15], v[52:55], v[84:87], v[12:15]\n"
    "global_load_dwordx4 v[44:47], v116, s[42:43] offset:3072 sc1\n"
    "s_waitcnt lgkmcnt(6)\n"
    "v_mfma_f32_16x16x32_bf16 v[16:19], v[48:51], v[88:91], v[16:19]\n"
    "s_add_u32 s39, s39, 1\n"
    "s_xor_b32 s40, s39, s59\n"
    "s_add_u32 s40, s40, s62\n"
    "v_mfma_f32_16x16x32_bf16 v[20:23], v[52:55], v[88:91], v[20:23]\n"
    "s_and_b32 s40, s40, 31\n"
    "s_lshl_b32 s40, s40, 12\n"
    "v_mfma_f32_16x16x32_bf16 v[24:27], v[48:51], v[92:95], v[24:27]\n"
    "s_add_u32 s42, s46, s40\n"
    "s_addc_u32 s43, s47, 0\n"
    "v_mfma_f32_16x16x32_bf16 v[28:31], v[52:55], v[92:95], v[28:31]\n"
    "s_add_u32 s37, s37, 1\n"
    "s_min_u32 s37, s37, 32\n"
    "s_waitcnt vmcnt(8) lgkmcnt(0)\n"
    "v_readfirstlane_b32 s38, v114\n"
    "v_readfirstlane_b32 s41, v115\n"
    "v_mfma_f32_16x16x32_bf16 v[0:3], v[56:59], v[96:99], v[0:3]\n"
    "s_min_u32 s38, s38, s41\n"
    "s_cmp_ge_u32 s38, s37\n"
    "v_mfma_f32_16x16x32_bf16 v[4:7], v[60:63], v[96:99], v[4:7]\n"
    "s_cbranch_scc0 .Lgs1\n"
    ".Lgr1:\n"
    "v_mfma_f32_16x16x32_bf16 v[8:11], v[56:59], v[100:103], v[8:11]\n"
    "ds_read_b128 v[80:83], v112 offset:16384\n"
    "ds_read_b128 v[84:87], v112 offset:18432\n"
    "v_mfma_f32_16x16x32_bf16 v[12:15], v[60:63], v[100:103], v[12:15]\n"
    "ds_read_b128 v[88:91], v112 offset:20480\n"
    "ds_read_b128 v[92:95], v112 offset:22528\n"
    "v_mfma_f32_16x16x32_bf16 v[16:19], v[56:59], v[104:107], v[16:19]\n"
    "ds_read_b128 v[96:99], v113 offset:16384\n"
    "ds_read_b128 v[100:103], v113 offset:18432\n"
    "v_mfma_f32_16x16x32_bf16 v[20:23], v[60:63], v[104:107], v[20:23]\n"
    "global_load_dwordx4 v[48:51], v116, s[42:43] offset:0 sc1\n"
    "v_mfma_f32_16x16x32_bf16 v[24:27], v[56:59], v[108:111], v[24:27]\n"
    "global_load_dwordx4 v[52:55], v116, s[42:43] offset:1024 sc1\n"
    "v_mfma_f32_16x16x32_bf16 v[28:31], v[60:63], v[108:111], v[28:31]\n"
    "s_cmp_eq_u32 s39, 34\n"
    "s_cbranch_scc1 .Lgx\n"
    "s_waitcnt vmcnt(8) lgkmcnt(4)\n"
    "v_mfma_f32_16x16x32_bf16 v[0:3], v[64:67], v[80:83], v[0:3]\n"
    "ds_read_b128 v[104:107], v113 offset:20480\n"
    "ds_read_b128 v[108:111], v113 offset:22528\n"
    "v_mfma_f32_16x16x32_bf16 v[4:7], v[68:71], v[80:83], v[4:7]\n"
    "s_mov_b64 exec, 1\n"
    "ds_add_u32 v119, v118 offset:32\n"
    "s_mov_b64 exec, -1\n"
    "ds_read_b64 v[114:115], v119 offset:16\n"
    "v_mfma_f32_16x16x32_bf16 v[8:11], v[64:67], v[84:87], v[8:11]\n"
    "ds_read_b32 v117, v125\n"
    "global_load_dwordx4 v[56:59], v116, s[42:43] offset:2048 sc1\n"
    "v_mfma_f32_16x16x32_bf16 v[12:15], v[68:71], v[84:87], v[12:15]\n"
    "global_load_dwordx4 v[60:63], v116, s[42:43] offset:3072 sc1\n"
    "s_waitcnt lgkmcnt(7)\n"
    "v_mfma_f32_16x16x32_bf16 v[16:19], v[64:67], v[88:91], v[16:19]\n"
    "s_add_u32 s39, s39, 1\n"
    "s_xor_b32 s40, s39, s59\n"
    "s_add_u32 s40, s40, s62\n"
    "v_mfma_f32_16x16x32_bf16 v[20:23], v[68:71], v[88:91], v[20:23]\n"
    "s_and_b32 s40, s40, 31\n"
    "s_lshl_b32 s40, s40, 12\n"
    "v_mfma_f32_16x16x32_bf16 v[24:27], v[64:67], v[92:95], v[24:27]\n"
    "s_add_u32 s42, s46, s40\n"
    "s_addc_u32 s43, s47, 0\n"
    "v_mfma_f32_16x16x32_bf16 v[28:31], v[68:71], v[92:95], v[28:31]\n"
    "s_add_u32 s37, s37, 1\n"
    "s_min_u32 s37, s37, 32\n"
    "s_waitcnt vmcnt(8) lgkmcnt(0)\n"
    "v_readfirstlane_b32 s38, v114\n"
    "v_readfirstlane_b32 s41, v115\n"
    "v_readfirstlane_b32 s44, v117\n"
    "v_mfma_f32_16x16x32_bf16 v[0:3], v[72:75], v[96:99], v[0:3]\n"
    "s_min_u32 s38, s38, s41\n"
    "s_cmp_ge_u32 s38, s37\n"
    "v_mfma_f32_16x16x32_bf16 v[4:7], v[76:79], v[96:99], v[4:7]\n"
    "s_cbranch_scc0 .Lgs2\n"
    ".Lgr2:\n"
    "v_mfma_f32_16x16x32_bf16 v[8:11], v[72:75], v[100:103], v[8:11]\n"
    "ds_read_b128 v[80:83], v126 offset:0\n"
    "ds_read_b128 v[84:87], v126 offset:2048\n"
    "v_mfma_f32_16x16x32_bf16 v[12:15], v[76:79], v[100:103], v[12:15]\n"
    "ds_read_b128 v[88:91], v126 offset:4096\n"
    "ds_read_b128 v[92:95], v126 offset:6144\n"
    "v_mfma_f32_16x16x32_bf16 v[16:19], v[72:75], v[104:107], v[16:19]\n"
    "ds_read_b128 v[96:99], v127 offset:0\n"
    "v_mfma_f32_16x16x32_bf16 v[20:23], v[76:79], v[104:107], v[20:23]\n"
    "ds_read_b128 v[100:103], v127 offset:2048\n"
    "v_mfma_f32_16x16x32_bf16 v[24:27], v[72:75], v[108:111], v[24:27]\n"
    "global_load_dwordx4 v[64:67], v116, s[42:43] offset:0 sc1\n"
    "v_mfma_f32_16x16x32_bf16 v[28:31], v[76:79], v[108:111], v[28:31]\n"
    "global_load_dwordx4 v[68:71], v116, s[42:43] offset:1024 sc1\n"
    "s_mul_i32 s45, s39, 2460\n"
    "s_add_i32 s45, s45, 26540\n"
    "s_setprio 2\n"
    "s_cmp_gt_i32 s44, s45\n"
    "s_cbranch_scc0 .Lgc2\n"
    "s_setprio 3\n"
    ".Lgc2:\n"
    "s_waitcnt vmcnt(8) lgkmcnt(4)\n"
    "v_mfma_f32_16x16x32_bf16 v[0:3], v[32:35], v[80:83], v[0:3]\n"
    "ds_read_b128 v[104:107], v127 offset:4096\n"
    "ds_read_b128 v[108:111], v127 offset:6144\n"
    "v_mfma_f32_16x16x32_bf16 v[4:7], v[36:39], v[80:83], v[4:7]\n"
    "s_mov_b64 exec, 1\n"
    "ds_add_u32 v119, v118 offset:36\n"
    "s_mov_b64 exec, -1\n"
    "ds_read_b64 v[114:115], v119 offset:16\n"
    "v_mfma_f32_16x16x32_bf16 v[8:11], v[32:35], v[84:87], v[8:11]\n"
    "global_load_dwordx4 v[72:75], v116, s[42:43] offset:2048 sc1\n"
    "v_mfma_f32_16x16x32_bf16 v[12:15], v[36:39], v[84:87], v[12:15]\n"
    "global_load_dwordx4 v[76:79], v116, s[42:43] offset:3072 sc1\n"
    "s_waitcnt lgkmcnt(6)\n"
    "v_mfma_f32_16x16x32_bf16 v[16:19], v[32:35], v[88:91], v[16:19]\n"
    "s_add_u32 s39, s39, 1\n"
    "s_xor_b32 s40, s39, s59\n"
    "s_add_u32 s40, s40, s62\n"
    "v_mfma_f32_16x16x32_bf16 v[20:23], v[36:39], v[88:91], v[20:23]\n"
    "s_and_b32 s40, s40, 31\n"
    "s_lshl_b32 s40, s40, 12\n"
    "v_mfma_f32_16x16x32_bf16 v[24:27], v[32:35], v[92:95], v[24:27]\n"
    "s_add_u32 s42, s46, s40\n"
    "s_addc_u32 s43, s47, 0\n"
    "v_mfma_f32_16x16x32_bf16 v[28:31], v[36:39], v[92:95], v[28:31]\n"
    "s_add_u32 s37, s37, 1\n"
    "s_min_u32 s37, s37, 32\n"
    "s_waitcnt vmcnt(8) lgkmcnt(0)\n"
    "v_readfirstlane_b32 s38, v114\n"
    "v_readfirstlane_b32 s41, v115\n"
    "v_mfma_f32_16x16x32_bf16 v[0:3], v[40:43], v[96:99], v[0:3]\n"
    "s_min_u32 s38, s38, s41\n"
    "s_cmp_ge_u32 s38, s37\n"
    "v_mfma_f32_16x16x32_bf16 v[4:7], v[44:47], v[96:99], v[4:7]\n"
    "s_cbranch_scc0 .Lgs3\n"
    ".Lgr3:\n"
    "v_mfma_f32_16x16x32_bf16 v[8:11], v[40:43], v[100:103], v[8:11]\n"
    "ds_read_b128 v[80:83], v126 offset:8192\n"
    "ds_read_b128 v[84:87], v126 offset:10240\n"
    "v_mfma_f32_16x16x32_bf16 v[12:15], v[44:47], v[100:103], v[12:15]\n"
    "ds_read_b128 v[88:91], v126 offset:12288\n"
    "ds_read_b128 v[92:95], v126 offset:14336\n"
    "v_mfma_f32_16x16x32_bf16 v[16:19], v[40:43], v[104:107], v[16:19]\n"
    "ds_read_b128 v[96:99], v127 offset:8192\n"
    "v_mfma_f32_16x16x32_bf16 v[20:23], v[44:47], v[104:107], v[20:23]\n"
    "ds_read_b128 v[100:103], v127 offset:10240\n"
    "v_mfma_f32_16x16x32_bf16 v[24:27], v[40:43], v[108:111], v[24:27]\n"
    "global_load_dwordx4 v[32:35], v116, s[42:43] offset:0 sc1\n"
    "v_mfma_f32_16x16x32_bf16 v[28:31], v[44:47], v[108:111], v[28:31]\n"
    "global_load_dwordx4 v[36:39], v116, s[42:43] offset:1024 sc1\n"
    "s_waitcnt vmcnt(8) lgkmcnt(4)\n"
    "v_mfma_f32_16x16x32_bf16 v[0:3], v[48:51], v[80:83], v[0:3]\n"
    "ds_read_b128 v[104:107], v127 offset:12288\n"
    "ds_read_b128 v[108:111], v127 offset:14336\n"
    "v_mfma_f32_16x16x32_bf16 v[4:7], v[52:55], v[80:83], v[4:7]\n"
    "s_mov_b64 exec, 1\n"
    "ds_add_u32 v119, v118 offset:40\n"
    "s_mov_b64 exec, -1\n"
    "ds_read_b64 v[114:115], v119 offset:16\n"
    "v_mfma_f32_16x16x32_bf16 v[8:11], v[48:51], v[84:87], v[8:11]\n"
    "ds_read_b32 v117, v125\n"
    "global_load_dwordx4 v[40:43], v116, s[42:43] offset:2048 sc1\n"
    "v_mfma_f32_16x16x32_bf16 v[12:15], v[52:55], v[84:87], v[12:15]\n"
    "global_load_dwordx4 v[44:47], v116, s[42:43] offset:3072 sc1\n"
    "s_waitcnt lgkmcnt(7)\n"
    "v_mfma_f32_16x16x32_bf16 v[16:19], v[48:51], v[88:91], v[16:19]\n"
    "s_add_u32 s39, s39, 1\n"
    "s_xor_b32 s40, s39, s59\n"
    "s_add_u32 s40, s40, s62\n"
    "v_mfma_f32_16x16x32_bf16 v[20:23], v[52:55], v[88:91], v[20:23]\n"
    "s_and_b32 s40, s40, 31\n"
    "s_lshl_b32 s40, s40, 12\n"
    "v_mfma_f32_16x16x32_bf16 v[24:27], v[48:51], v[92:95], v[24:27]\n"
    "s_add_u32 s42, s46, s40\n"
    "s_addc_u32 s43, s47, 0\n"
    "v_mfma_f32_16x16x32_bf16 v[28:31], v[52:55], v[92:95], v[28:31]\n"
    "s_add_u32 s37, s37, 1\n"
    "s_min_u32 s37, s37, 32\n"
    "s_waitcnt vmcnt(8) lgkmcnt(0)\n"
    "v_readfirstlane_b32 s38, v114\n"
    "v_readfirstlane_b32 s41, v115\n"
    "v_readfirstlane_b32 s44, v117\n"
    "v_mfma_f32_16x16x32_bf16 v[0:3], v[56:59], v[96:99], v[0:3]\n"
    "s_min_u32 s38, s38, s41\n"
    "s_cmp_ge_u32 s38, s37\n"
    "v_mfma_f32_16x16x32_bf16 v[4:7], v[60:63], v[96:99], v[4:7]\n"
    "s_cbranch_scc0 .Lgs4\n"
    ".Lgr4:\n"
    "v_mfma_f32_16x16x32_bf16 v[8:11], v[56:59], v[100:103], v[8:11]\n"
    "ds_read_b128 v[80:83], v126 offset:16384\n"
    "ds_read_b128 v[84:87], v126 offset:18432\n"
    "v_mfma_f32_16x16x32_bf16 v[12:15], v[60:63], v[100:103], v[12:15]\n"
    "ds_read_b128 v[88:91], v126 offset:20480\n"
    "ds_read_b128 v[92:95], v126 offset:22528\n"
    "v_mfma_f32_16x16x32_bf16 v[16:19], v[56:59], v[104:107], v[16:19]\n"
    "ds_read_b128 v[96:99], v127 offset:16384\n"
    "ds_read_b128 v[100:103], v127 offset:18432\n"
    "v_mfma_f32_16x16x32_bf16 v[20:23], v[60:63], v[104:107], v[20:23]\n"
    "global_load_dwordx4 v[48:51], v116, s[42:43] offset:0 sc1\n"
    "v_mfma_f32_16x16x32_bf16 v[24:27], v[56:59], v[108:111], v[24:27]\n"
    "global_load_dwordx4 v[52:55], v116, s[42:43] offset:1024 sc1\n"
    "v_mfma_f32_16x16x32_bf16 v[28:31], v[60:63], v[108:111], v[28:31]\n"
    "s_cmp_eq_u32 s39, 31\n"
    "s_cbranch_scc1 .Lgx\n"
    ".Lgy:\n"
    "s_mul_i32 s45, s39, 2460\n"
    "s_add_i32 s45, s45, 26540\n"
    "s_setprio 2\n"
    "s_cmp_gt_i32 s44, s45\n"
    "s_cbranch_scc0 .Lgc4\n"
    "s_setprio 3\n"
    ".Lgc4:\n"
    "s_waitcnt vmcnt(8) lgkmcnt(4)\n"
    "v_mfma_f32_16x16x32_bf16 v[0:3], v[64:67], v[80:83], v[0:3]\n"
    "ds_read_b128 v[104:107], v127 offset:20480\n"
    "ds_read_b128 v[108:111], v127 offset:22528\n"
    "v_mfma_f32_16x16x32_bf16 v[4:7], v[68:71], v[80:83], v[4:7]\n"
    "s_mov_b64 exec, 1\n"
    "ds_add_u32 v119, v118 offset:44\n"
    "s_mov_b64 exec, -1\n"
    "ds_read_b64 v[114:115], v119 offset:16\n"
    "v_mfma_f32_16x16x32_bf16 v[8:11], v[64:67], v[84:87], v[8:11]\n"
    "global_load_dwordx4 v[56:59], v116, s[42:43] offset:2048 sc1\n"
    "v_mfma_f32_16x16x32_bf16 v[12:15], v[68:71], v[84:87], v[12:15]\n"
    "global_load_dwordx4 v[60:63], v116, s[42:43] offset:3072 sc1\n"
    "s_waitcnt lgkmcnt(6)\n"
    "v_mfma_f32_16x16x32_bf16 v[16:19], v[64:67], v[88:91], v[16:19]\n"
    "s_add_u32 s39, s39, 1\n"
    "s_xor_b32 s40, s39, s59\n"
    "s_add_u32 s40, s40, s62\n"
    "v_mfma_f32_16x16x32_bf16 v[20:23], v[68:71], v[88:91], v[20:23]\n"
    "s_and_b32 s40, s40, 31\n"
    "s_lshl_b32 s40, s40, 12\n"
    "s_add_u32 s42, s46, s40\n"
    "v_mfma_f32_16x16x32_bf16 v[24:27], v[64:67], v[92:95], v[24:27]\n"
    "s_addc_u32 s43, s47, 0\n"
    "s_cmp_eq_u32 s39, 32\n"
    "s_cselect_b64 s[42:43], s[48:49], s[42:43]\n"
    "v_mfma_f32_16x16x32_bf16 v[28:31], v[68:71], v[92:95], v[28:31]\n"
    "s_add_u32 s37, s37, 1\n"
    "s_min_u32 s37, s37, 32\n"
    "s_waitcnt vmcnt(8) lgkmcnt(0)\n"
    "v_readfirstlane_b32 s38, v114\n"
    "v_readfirstlane_b32 s41, v115\n"
    "v_mfma_f32_16x16x32_bf16 v[0:3], v[72:75], v[96:99], v[0:3]\n"
    "s_min_u32 s38, s38, s41\n"
    "s_cmp_ge_u32 s38, s37\n"
    "v_mfma_f32_16x16x32_bf16 v[4:7], v[76:79], v[96:99], v[4:7]\n"
    "s_cbranch_scc0 .Lgs5\n"
    ".Lgr5:\n"
    "v_mfma_f32_16x16x32_bf16 v[8:11], v[72:75], v[100:103], v[8:11]\n"
    "ds_read_b128 v[80:83], v112 offset:0\n"
    "ds_read_b128 v[84:87], v112 offset:2048\n"
    "v_mfma_f32_16x16x32_bf16 v[12:15], v[76:79], v[100:103], v[12:15]\n"
    "ds_read_b128 v[88:91], v112 offset:4096\n"
    "ds_read_b128 v[92:95], v112 offset:6144\n"
    "v_mfma_f32_16x16x32_bf16 v[16:19], v[72:75], v[104:107], v[16:19]\n"
    "ds_read_b128 v[96:99], v113 offset:0\n"
    "v_mfma_f32_16x16x32_bf16 v[20:23], v[76:79], v[104:107], v[20:23]\n"
    "ds_read_b128 v[100:103], v113 offset:2048\n"
    "v_mfma_f32_16x16x32_bf16 v[24:27], v[72:75], v[108:111], v[24:27]\n"
    "global_load_dwordx4 v[64:67], v116, s[42:43] offset:0 sc1\n"
    "v_mfma_f32_16x16x32_bf16 v[28:31], v[76:79], v[108:111], v[28:31]\n"
    "global_load_dwordx4 v[68:71], v116, s[42:43] offset:1024 sc1\n"
    "s_branch .Lgl\n"
    ".Lgs0:\n"
    "s_setprio 3\n"
    ".Lw_119:\n"
    "ds_read_b64 v[114:115], v119 offset:16\n"
    "s_waitcnt lgkmcnt(0)\n"
    "v_min_u32 v117, v114, v115\n"
    "s_nop 0\n"
    "v_readfirstlane_b32 s38, v117\n"
    "s_cmp_ge_u32 s38, s37\n"
    "s_cbranch_scc1 .Ld_120\n"
    "s_sleep 1\n"
    "s_add_u32 s63, s63, 1\n"
    "s_bitcmp1_b32 s63, 19\n"
    "s_cbranch_scc0 .Lw_119\n"
    "s_sub_u32 s63, s63, 1\n"
    ".Ld_120:\n"
    "s_branch .Lgr0\n"
    ".Lgs1:\n"
    "s_setprio 3\n"
    ".Lw_121:\n"
    "ds_read_b64 v[114:115], v119 offset:16\n"
    "s_waitcnt lgkmcnt(0)\n"
    "v_min_u32 v117, v114, v115\n"
    "s_nop 0\n"
    "v_readfirstlane_b32 s38, v117\n"
    "s_cmp_ge_u32 s38, s37\n"
    "s_cbranch_scc1 .Ld_122\n"
    "s_sleep 1\n"
    "s_add_u32 s63, s63, 1\n"
    "s_bitcmp1_b32 s63, 19\n"
    "s_cbranch_scc0 .Lw_121\n"
    "s_sub_u32 s63, s63, 1\n"
    ".Ld_122:\n"
    "s_branch .Lgr1\n"
    ".Lgs2:\n"
    "s_setprio 3\n"
    ".Lw_123:\n"
    "ds_read_b64 v[114:115], v119 offset:16\n"
    "s_waitcnt lgkmcnt(0)\n"
    "v_min_u32 v117, v114, v115\n"
    "s_nop 0\n"
    "v_readfirstlane_b32 s38, v117\n"
    "s_cmp_ge_u32 s38, s37\n"
    "s_cbranch_scc1 .Ld_124\n"
    "s_sleep 1\n"
    "s_add_u32 s63, s63, 1\n"
    "s_bitcmp1_b32 s63, 19\n"
    "s_cbranch_scc0 .Lw_123\n"
    "s_sub_u32 s63, s63, 1\n"
    ".Ld_124:\n"
    "s_branch .Lgr2\n"
    ".Lgs3:\n"
    "s_setprio 3\n"
    ".Lw_125:\n"
    "ds_read_b64 v[114:115], v119 offset:16\n"
    "s_waitcnt lgkmcnt(0)\n"
    "v_min_u32 v117, v114, v115\n"
    "s_nop 0\n"
    "v_readfirstlane_b32 s38, v117\n"
    "s_cmp_ge_u32 s38, s37\n"
    "s_cbranch_scc1 .Ld_126\n"
    "s_sleep 1\n"
    "s_add_u32 s63, s63, 1\n"
    "s_bitcmp1_b32 s63, 19\n"
    "s_cbranch_scc0 .Lw_125\n"
    "s_sub_u32 s63, s63, 1\n"
    ".Ld_126:\n"
    "s_branch .Lgr3\n"
    ".Lgs4:\n"
    "s_setprio 3\n"
    ".Lw_127:\n"
    "ds_read_b64 v[114:115], v119 offset:16\n"
    "s_waitcnt lgkmcnt(0)\n"
    "v_min_u32 v117, v114, v115\n"
    "s_nop 0\n"
    "v_readfirstlane_b32 s38, v117\n"
    "s_cmp_ge_u32 s38, s37\n"
    "s_cbranch_scc1 .Ld_128\n"
    "s_sleep 1\n"
    "s_add_u32 s63, s63, 1\n"
    "s_bitcmp1_b32 s63, 19\n"
    "s_cbranch_scc0 .Lw_127\n"
    "s_sub_u32 s63, s63, 1\n"
    ".Ld_128:\n"
    "s_branch .Lgr4\n"
    ".Lgs5:\n"
    "s_setprio 3\n"
    ".Lw_129:\n"
    "ds_read_b64 v[114:115], v119 offset:16\n"
    "s_waitcnt lgkmcnt(0)\n"
    "v_min_u32 v117, v114, v115\n"
    "s_nop 0\n"
    "v_readfirstlane_b32 s38, v117\n"
    "s_cmp_ge_u32 s38, s37\n"
    "s_cbranch_scc1 .Ld_130\n"
    "s_sleep 1\n"
    "s_add_u32 s63, s63, 1\n"
    "s_bitcmp1_b32 s63, 19\n"
    "s_cbranch_scc0 .Lw_129\n"
    "s_sub_u32 s63, s63, 1\n"
    ".Ld_130:\n"
    "s_branch .Lgr5\n"
    ".Lgx:\n"
    "s_setprio 3\n"
    "s_cmp_eq_u32 s39, 34\n"
    "s_cbranch_scc1 .Lge\n"
    ".Lw_131:\n"
    "ds_read_b32 v117, v119 offset:112\n"
    "s_waitcnt lgkmcnt(0)\n"
    "v_readfirstlane_b32 s60, v117\n"
    "s_cmp_ge_u32 s60, 1\n"
    "s_cbranch_scc1 .Ld_132\n"
    "s_sleep 1\n"
    "s_add_u32 s63, s63, 1\n"
    "s_bitcmp1_b32 s63, 19\n"
    "s_cbranch_scc0 .Lw_131\n"
    "s_sub_u32 s63, s63, 1\n"
    ".Ld_132:\n"
    "s_setprio 2\n"
    "s_branch .Lgy\n"
    ".Lge:\n"
    "s_waitcnt vmcnt(6)\n"
    "s_waitcnt lgkmcnt(0)\n"
    "s_mov_b64 exec, 1\n"
    "v_mov_b32 v117, 1\n"
    "ds_add_u32 v119, v117 offset:128\n"
    "s_mov_b64 exec, -1\n"
    "v_lshlrev_b32 v80, 16, v64\n"
    "v_lshlrev_b32 v82, 16, v65\n"
    "v_lshlrev_b32 v84, 16, v66\n"
    "v_lshlrev_b32 v86, 16, v67\n"
    "v_and_b32 v81, 0xffff0000, v64\n"
    "v_and_b32 v83, 0xffff0000, v65\n"
    "v_and_b32 v85, 0xffff0000, v66\n"
    "v_and_b32 v87, 0xffff0000, v67\n"
    "v_pk_add_f32 v[0:1], v[0:1], v[120:121] op_sel:[0,0] op_sel_hi:[1,0]\n"
    "v_pk_add_f32 v[2:3], v[2:3], v[120:121] op_sel:[0,0] op_sel_hi:[1,0]\n"
    "v_pk_add_f32 v[4:5], v[4:5], v[120:121] op_sel:[0,0] op_sel_hi:[1,0]\n"
    "v_pk_add_f32 v[6:7], v[6:7], v[120:121] op_sel:[0,0] op_sel_hi:[1,0]\n"
    "v_pk_mul_f32 v[0:1], v[0:1], v[80:81]\n"
    "v_pk_mul_f32 v[2:3], v[2:3], v[82:83]\n"
    "v_pk_mul_f32 v[4:5], v[4:5], v[84:85]\n"
    "v_pk_mul_f32 v[6:7], v[6:7], v[86:87]\n"
    "v_cvt_pk_bf16_f32 v64, v0, v1\n"
    "v_cvt_pk_bf16_f32 v65, v2, v3\n"
    "v_cvt_pk_bf16_f32 v66, v4, v5\n"
    "v_cvt_pk_bf16_f32 v67, v6, v7\n"
    "global_store_dwordx4 v124, v[64:67], s[50:51] offset:0 sc1\n"
    "v_lshlrev_b32 v80, 16, v68\n"
    "v_lshlrev_b32 v82, 16, v69\n"
    "v_lshlrev_b32 v84, 16, v70\n"
    "v_lshlrev_b32 v86, 16, v71\n"
    "v_and_b32 v81, 0xffff0000, v68\n"
    "v_and_b32 v83, 0xffff0000, v69\n"
    "v_and_b32 v85, 0xffff0000, v70\n"
    "v_and_b32 v87, 0xffff0000, v71\n"
    "v_pk_add_f32 v[8:9], v[8:9], v[120:121] op_sel:[0,1] op_sel_hi:[1,1]\n"
    "v_pk_add_f32 v[10:11], v[10:11], v[120:121] op_sel:[0,1] op_sel_hi:[1,1]\n"
    "v_pk_add_f32 v[12:13], v[12:13], v[120:121] op_sel:[0,1] op_sel_hi:[1,1]\n"
    "v_pk_add_f32 v[14:15], v[14:15], v[120:121] op_sel:[0,1] op_sel_hi:[1,1]\n"
    "v_pk_mul_f32 v[8:9], v[8:9], v[80:81]\n"
    "v_pk_mul_f32 v[10:11], v[10:11], v[82:83]\n"
    "v_pk_mul_f32 v[12:13], v[12:13], v[84:85]\n"
    "v_pk_mul_f32 v[14:15], v[14:15], v[86:87]\n"
    "v_cvt_pk_bf16_f32 v68, v8, v9\n"
    "v_cvt_pk_bf16_f32 v69, v10, v11\n"
    "v_cvt_pk_bf16_f32 v70, v12, v13\n"
    "v_cvt_pk_bf16_f32 v71, v14, v15\n"
    "global_store_dwordx4 v124, v[68:71], s[50:51] offset:1024 sc1\n"
    "v_lshlrev_b32 v80, 16, v72\n"
    "v_lshlrev_b32 v82, 16, v73\n"
    "v_lshlrev_b32 v84, 16, v74\n"
    "v_lshlrev_b32 v86, 16, v75\n"
    "v_and_b32 v81, 0xffff0000, v72\n"
    "v_and_b32 v83, 0xffff0000, v73\n"
    "v_and_b32 v85, 0xffff0000, v74\n"
    "v_and_b32 v87, 0xffff0000, v75\n"
    "v_pk_add_f32 v[16:17], v[16:17], v[122:123] op_sel:[0,0] op_sel_hi:[1,0]\n"
    "v_pk_add_f32 v[18:19], v[18:19], v[122:123] op_sel:[0,0] op_sel_hi:[1,0]\n"
    "v_pk_add_f32 v[20:21], v[20:21], v[122:123] op_sel:[0,0] op_sel_hi:[1,0]\n"
    "v_pk_add_f32 v[22:23], v[22:23], v[122:123] op_sel:[0,0] op_sel_hi:[1,0]\n"
    "v_pk_mul_f32 v[16:17], v[16:17], v[80:81]\n"
    "v_pk_mul_f32 v[18:19], v[18:19], v[82:83]\n"
    "v_pk_mul_f32 v[20:21], v[20:21], v[84:85]\n"
    "v_pk_mul_f32 v[22:23], v[22:23], v[86:87]\n"
    "v_cvt_pk_bf16_f32 v72, v16, v17\n"
    "v_cvt_pk_bf16_f32 v73, v18, v19\n"
    "v_cvt_pk_bf16_f32 v74, v20, v21\n"
    "v_cvt_pk_bf16_f32 v75, v22, v23\n"
    "global_store_dwordx4 v124, v[72:75], s[50:51] offset:2048 sc1\n"
    "v_lshlrev_b32 v80, 16, v76\n"
    "v_lshlrev_b32 v82, 16, v77\n"
    "v_lshlrev_b32 v84, 16, v78\n"
    "v_lshlrev_b32 v86, 16, v79\n"
    "v_and_b32 v81, 0xffff0000, v76\n"
    "v_and_b32 v83, 0xffff0000, v77\n"
    "v_and_b32 v85, 0xffff0000, v78\n"
    "v_and_b32 v87, 0xffff0000, v79\n"
    "v_pk_add_f32 v[24:25], v[24:25], v[122:123] op_sel:[0,1] op_sel_hi:[1,1]\n"
    "v_pk_add_f32 v[26:27], v[26:27], v[122:123] op_sel:[0,1] op_sel_hi:[1,1]\n"
    "v_pk_add_f32 v[28:29], v[28:29], v[122:123] op_sel:[0,1] op_sel_hi:[1,1]\n"
    "v_pk_add_f32 v[30:31], v[30:31], v[122:123] op_sel:[0,1] op_sel_hi:[1,1]\n"
    "v_pk_mul_f32 v[24:25], v[24:25], v[80:81]\n"
    "v_pk_mul_f32 v[26:27], v[26:27], v[82:83]\n"
    "v_pk_mul_f32 v[28:29], v[28:29], v[84:85]\n"
    "v_pk_mul_f32 v[30:31], v[30:31], v[86:87]\n"
    "v_cvt_pk_bf16_f32 v76, v24, v25\n"
    "v_cvt_pk_bf16_f32 v77, v26, v27\n"
    "v_cvt_pk_bf16_f32 v78, v28, v29\n"
    "v_cvt_pk_bf16_f32 v79, v30, v31\n"
    "global_store_dwordx4 v124, v[76:79], s[50:51] offset:3072 sc1\n"
    "v_lshrrev_b32 v125, 1, v124\n"
    "v_add_u32 v125, s57, v125\n"
    "v_mov_b32 v126, s34\n"
    "v_mov_b32 v127, s35\n"
    "s_waitcnt vmcnt(0)\n"
    "s_mov_b64 exec, 0xffff\n"
    "global_store_dwordx2 v125, v[126:127], s[10:11] sc1\n"
    "s_mov_b64 exec, -1\n"
    "s_waitcnt lgkmcnt(0)\n"
    "s_mov_b64 exec, 1\n"
    "v_mov_b32 v117, 1\n"
    "ds_add_u32 v119, v117 offset:132\n"
    "s_mov_b64 exec, -1\n"
    "v_lshrrev_b32 v0, 4, v124\n"
    "s_branch .Lfin\n"
    ".Lfin0:\n"
    "s_waitcnt lgkmcnt(0)\n"
    "s_mov_b64 exec, 1\n"
    "v_mov_b32 v117, 1\n"
    "ds_add_u32 v119, v117 offset:128\n"
    "s_mov_b64 exec, -1\n"
    "s_waitcnt lgkmcnt(0)\n"
    "s_mov_b64 exec, 1\n"
    "v_mov_b32 v117, 1\n"
    "ds_add_u32 v119, v117 offset:132\n"
    "s_mov_b64 exec, -1\n"
    ".Lw_133:\n"
    "ds_read_b32 v117, v119 offset:116\n"
    "s_waitcnt lgkmcnt(0)\n"
    "v_readfirstlane_b32 s60, v117\n"
    "s_cmp_ge_u32 s60, 8\n"
    "s_cbranch_scc1 .Ld_134\n"
    "s_sleep 8\n"
    "s_add_u32 s63, s63, 1\n"
    "s_bitcmp1_b32 s63, 19\n"
    "s_cbranch_scc0 .Lw_133\n"
    "s_sub_u32 s63, s63, 1\n"
    ".Ld_134:\n"
    ".Lfin:\n"
    "v_and_b32 v1, 15, v0\n"
    "v_lshrrev_b32 v2, 4, v0\n"
    "v_lshlrev_b32 v112, 4, v1\n"
    "v_lshl_or_b32 v112, v2, 9, v112\n"
    "v_add_u32 v113, 0x10000, v112\n"
    "v_add_u32 v114, 0x20000, v112\n"
    "v_lshlrev_b32 v115, 10, v1\n"
    "v_lshl_or_b32 v116, v2, 3, v115\n"
    "v_and_b32 v3, 1, v2\n"
    "v_lshl_or_b32 v115, v3, 5, v115\n"
    "v_lshrrev_b32 v3, 1, v2\n"
    "v_lshl_or_b32 v115, v3, 4, v115\n"
    "s_lshl2_add_u32 s40, s53, s55\n"
    "s_lshl_b32 s40, s40, 7\n"
    "s_add_u32 s40, s40, s54\n"
    "s_lshl_b32 s40, s40, 7\n"
    "s_add_u32 s42, s4, s40\n"
    "s_addc_u32 s43, s5, 0\n"
    "s_add_u32 s46, s20, s40\n"
    "s_addc_u32 s47, s21, 0\n"
    "global_load_dwordx2 v[120:121], v116, s[42:43] offset:0\n"
    "global_load_dwordx2 v[122:123], v116, s[42:43] offset:32\n"
    "global_load_dwordx2 v[124:125], v116, s[42:43] offset:64\n"
    "global_load_dwordx2 v[126:127], v116, s[42:43] offset:96\n"
    "s_mul_i32 s44, s53, 112\n"
    "s_add_u32 s44, s44, s55\n"
    "s_lshl_b32 s44, s44, 10\n"
    "s_add_u32 s44, s44, 18878464\n"
    "s_add_u32 s48, s10, s44\n"
    "s_addc_u32 s49, s11, 0\n"
    "s_lshr_b32 s40, s55, 1\n"
    "s_lshl1_add_u32 s44, s53, s40\n"
    "s_lshl_b32 s44, s44, 5\n"
    "s_and_b32 s41, s55, 1\n"
    "s_add_u32 s44, s44, s41\n"
    "s_lshl_b32 s44, s44, 10\n"
    "s_add_u32 s44, s44, 4096\n"
    "s_add_u32 s50, s10, s44\n"
    "s_addc_u32 s51, s11, 0\n"
    "v_lshlrev_b32 v118, 4, v0\n"
    "s_mov_b32 s38, 0\n"
    "s_mov_b64 s[44:45], s[48:49]\n"
    "s_mov_b64 s[58:59], s[50:51]\n"
    "s_mov_b32 s37, 0\n"
    "s_cmp_ge_u32 s38, 6\n"
    "s_cbranch_scc0 .Ln_135\n"
    ".Lb_136:\n"
    "ds_read_b128 v[16:19], v112 offset:24576\n"
    "ds_read_b128 v[20:23], v112 offset:24832\n"
    "ds_read_b128 v[24:27], v112 offset:28672\n"
    "ds_read_b128 v[28:31], v112 offset:28928\n"
    "ds_read_b128 v[32:35], v112 offset:26624\n"
    "ds_read_b128 v[36:39], v112 offset:26880\n"
    "ds_read_b128 v[40:43], v112 offset:30720\n"
    "ds_read_b128 v[44:47], v112 offset:30976\n"
    ".Lw_137:\n"
    "ds_read_b32 v117, v119 offset:136\n"
    "s_waitcnt lgkmcnt(0)\n"
    "v_readfirstlane_b32 s60, v117\n"
    "s_cmp_ge_u32 s60, 1\n"
    "s_cbranch_scc1 .Ld_138\n"
    "s_sleep 2\n"
    "s_add_u32 s63, s63, 1\n"
    "s_bitcmp1_b32 s63, 19\n"
    "s_cbranch_scc0 .Lw_137\n"
    "s_sub_u32 s63, s63, 1\n"
    ".Ld_138:\n"
    "global_load_dwordx4 v[64:67], v118, s[48:49] offset:-4096 sc1\n"
    "global_load_dwordx4 v[68:71], v118, s[48:49] offset:0 sc1\n"
    "s_add_u32 s48, s48, 0x2000\n"
    "s_addc_u32 s49, s49, 0\n"
    "global_load_dwordx4 v[72:75], v118, s[48:49] offset:-4096 sc1\n"
    "global_load_dwordx4 v[76:79], v118, s[48:49] offset:0 sc1\n"
    "s_add_u32 s48, s48, 0x2000\n"
    "s_addc_u32 s49, s49, 0\n"
    "global_load_dwordx4 v[80:83], v118, s[48:49] offset:-4096 sc1\n"
    "global_load_dwordx4 v[84:87], v118, s[48:49] offset:0 sc1\n"
    "s_add_u32 s48, s48, 0x2000\n"
    "s_addc_u32 s49, s49, 0\n"
    "global_load_dwordx4 v[88:91], v118, s[48:49] offset:-4096 sc1\n"
    "global_load_dwordx4 v[92:95], v118, s[48:49] offset:0 sc1\n"
    "s_add_u32 s48, s48, 0x2000\n"
    "s_addc_u32 s49, s49, 0\n"
    "global_load_dwordx4 v[96:99], v118, s[48:49] offset:-4096 sc1\n"
    "global_load_dwordx4 v[100:103], v118, s[48:49] offset:0 sc1\n"
    "s_add_u32 s48, s48, 0x2000\n"
    "s_addc_u32 s49, s49, 0\n"
    "global_load_dwordx4 v[104:107], v118, s[48:49] offset:-4096 sc1\n"
    "global_load_dwordx4 v[108:111], v118, s[48:49] offset:0 sc1\n"
    "s_add_u32 s48, s48, 0x2000\n"
    "s_addc_u32 s49, s49, 0\n"
    "s_setprio 3\n"
    "s_waitcnt vmcnt(11) lgkmcnt(4)\n"
    "v_mfma_f32_16x16x32_bf16 v[0:3], v[16:19], v[64:67], 0\n"
    "ds_read_b128 v[48:51], v112 offset:32768\n"
    "v_mfma_f32_16x16x32_bf16 v[4:7], v[20:23], v[64:67], 0\n"
    "ds_read_b128 v[52:55], v112 offset:33024\n"
    "v_mfma_f32_16x16x32_bf16 v[8:11], v[24:27], v[64:67], 0\n"
    "ds_read_b128 v[56:59], v112 offset:36864\n"
    "v_mfma_f32_16x16x32_bf16 v[12:15], v[28:31], v[64:67], 0\n"
    "ds_read_b128 v[60:63], v112 offset:37120\n"
    "s_waitcnt vmcnt(10) lgkmcnt(4)\n"
    "v_mfma_f32_16x16x32_bf16 v[0:3], v[32:35], v[68:71], v[0:3]\n"
    "ds_read_b128 v[16:19], v112 offset:34816\n"
    "ds_read_b128 v[20:23], v112 offset:35072\n"
    "v_mfma_f32_16x16x32_bf16 v[4:7], v[36:39], v[68:71], v[4:7]\n"
    "ds_read_b128 v[24:27], v112 offset:38912\n"
    "v_mfma_f32_16x16x32_bf16 v[8:11], v[40:43], v[68:71], v[8:11]\n"
    "ds_read_b128 v[28:31], v112 offset:39168\n"
    "v_mfma_f32_16x16x32_bf16 v[12:15], v[44:47], v[68:71], v[12:15]\n"
    "global_load_dwordx4 v[64:67], v118, s[48:49] offset:-4096 sc1\n"
    "s_waitcnt vmcnt(10) lgkmcnt(4)\n"
    "v_mfma_f32_16x16x32_bf16 v[0:3], v[48:51], v[72:75], v[0:3]\n"
    "ds_read_b128 v[32:35], v112 offset:40960\n"
    "ds_read_b128 v[36:39], v112 offset:41216\n"
    "v_mfma_f32_16x16x32_bf16 v[4:7], v[52:55], v[72:75], v[4:7]\n"
    "ds_read_b128 v[40:43], v112 offset:45056\n"
    "ds_read_b128 v[44:47], v112 offset:45312\n"
    "v_mfma_f32_16x16x32_bf16 v[8:11], v[56:59], v[72:75], v[8:11]\n"
    "global_load_dwordx4 v[68:71], v118, s[48:49] offset:0 sc1\n"
    "s_add_u32 s48, s48, 0x2000\n"
    "v_mfma_f32_16x16x32_bf16 v[12:15], v[60:63], v[72:75], v[12:15]\n"
    "s_addc_u32 s49, s49, 0\n"
    "s_waitcnt vmcnt(10) lgkmcnt(4)\n"
    "v_mfma_f32_16x16x32_bf16 v[0:3], v[16:19], v[76:79], v[0:3]\n"
    "ds_read_b128 v[48:51], v112 offset:43008\n"
    "ds_read_b128 v[52:55], v112 offset:43264\n"
    "v_mfma_f32_16x16x32_bf16 v[4:7], v[20:23], v[76:79], v[4:7]\n"
    "ds_read_b128 v[56:59], v112 offset:47104\n"
    "v_mfma_f32_16x16x32_bf16 v[8:11], v[24:27], v[76:79], v[8:11]\n"
    "ds_read_b128 v[60:63], v112 offset:47360\n"
    "v_mfma_f32_16x16x32_bf16 v[12:15], v[28:31], v[76:79], v[12:15]\n"
    "global_load_dwordx4 v[72:75], v118, s[48:49] offset:-4096 sc1\n"
    "s_mov_b64 exec, 1\n"
    "v_mov_b32 v117, 1\n"
    "ds_add_u32 v119, v117 offset:148\n"
    "s_mov_b64 exec, -1\n"
    "s_cmp_ge_u32 s38, 12\n"
    "s_cbranch_scc0 .Ln_139\n"
    ".Lb_140:\n"
    "s_waitcnt vmcnt(10) lgkmcnt(4)\n"
    "v_mfma_f32_16x16x32_bf16 v[0:3], v[32:35], v[80:83], v[0:3]\n"
    "ds_read_b128 v[16:19], v112 offset:49152\n"
    "ds_read_b128 v[20:23], v112 offset:49408\n"
    "v_mfma_f32_16x16x32_bf16 v[4:7], v[36:39], v[80:83], v[4:7]\n"
    "ds_read_b128 v[24:27], v112 offset:53248\n"
    "ds_read_b128 v[28:31], v112 offset:53504\n"
    "v_mfma_f32_16x16x32_bf16 v[8:11], v[40:43], v[80:83], v[8:11]\n"
    "global_load_dwordx4 v[76:79], v118, s[48:49] offset:0 sc1\n"
    "s_add_u32 s48, s48, 0x2000\n"
    "v_mfma_f32_16x16x32_bf16 v[12:15], v[44:47], v[80:83], v[12:15]\n"
    "s_addc_u32 s49, s49, 0\n"
    "s_waitcnt vmcnt(10) lgkmcnt(4)\n"
    "v_mfma_f32_16x16x32_bf16 v[0:3], v[48:51], v[84:87], v[0:3]\n"
    "ds_read_b128 v[32:35], v112 offset:51200\n"
    "ds_read_b128 v[36:39], v112 offset:51456\n"
    "v_mfma_f32_16x16x32_bf16 v[4:7], v[52:55], v[84:87], v[4:7]\n"
    "ds_read_b128 v[40:43], v112 offset:55296\n"
    "v_mfma_f32_16x16x32_bf16 v[8:11], v[56:59], v[84:87], v[8:11]\n"
    "ds_read_b128 v[44:47], v112 offset:55552\n"
    "v_mfma_f32_16x16x32_bf16 v[12:15], v[60:63], v[84:87], v[12:15]\n"
    "global_load_dwordx4 v[80:83], v118, s[48:49] offset:-4096 sc1\n"
    "s_waitcnt vmcnt(10) lgkmcnt(4)\n"
    "v_mfma_f32_16x16x32_bf16 v[0:3], v[16:19], v[88:91], v[0:3]\n"
    "ds_read_b128 v[48:51], v112 offset:57344\n"
    "ds_read_b128 v[52:55], v112 offset:57600\n"
    "v_mfma_f32_16x16x32_bf16 v[4:7], v[20:23], v[88:91], v[4:7]\n"
    "ds_read_b128 v[56:59], v112 offset:61440\n"
    "ds_read_b128 v[60:63], v112 offset:61696\n"
    "v_mfma_f32_16x16x32_bf16 v[8:11], v[24:27], v[88:91], v[8:11]\n"
    "global_load_dwordx4 v[84:87], v118, s[48:49] offset:0 sc1\n"
    "s_add_u32 s48, s48, 0x2000\n"
    "v_mfma_f32_16x16x32_bf16 v[12:15], v[28:31], v[88:91], v[12:15]\n"
    "s_addc_u32 s49, s49, 0\n"
    "s_waitcnt vmcnt(10) lgkmcnt(4)\n"
    "v_mfma_f32_16x16x32_bf16 v[0:3], v[32:35], v[92:95], v[0:3]\n"
    "ds_read_b128 v[16:19], v112 offset:59392\n"
    "ds_read_b128 v[20:23], v112 offset:59648\n"
    "v_mfma_f32_16x16x32_bf16 v[4:7], v[36:39], v[92:95], v[4:7]\n"
    "ds_read_b128 v[24:27], v112 offset:63488\n"
    "v_mfma_f32_16x16x32_bf16 v[8:11], v[40:43], v[92:95], v[8:11]\n"
    "ds_read_b128 v[28:31], v112 offset:63744\n"
    "v_mfma_f32_16x16x32_bf16 v[12:15], v[44:47], v[92:95], v[12:15]\n"
    "global_load_dwordx4 v[88:91], v118, s[48:49] offset:-4096 sc1\n"
    "s_waitcnt vmcnt(10) lgkmcnt(4)\n"
    "v_mfma_f32_16x16x32_bf16 v[0:3], v[48:51], v[96:99], v[0:3]\n"
    "ds_read_b128 v[32:35], v113 offset:0\n"
    "ds_read_b128 v[36:39], v113 offset:256\n"
    "v_mfma_f32_16x16x32_bf16 v[4:7], v[52:55], v[96:99], v[4:7]\n"
    "ds_read_b128 v[40:43], v113 offset:4096\n"
    "ds_read_b128 v[44:47], v113 offset:4352\n"
    "v_mfma_f32_16x16x32_bf16 v[8:11], v[56:59], v[96:99], v[8:11]\n"
    "global_load_dwordx4 v[92:95], v118, s[48:49] offset:0 sc1\n"
    "s_add_u32 s48, s48, 0x2000\n"
    "v_mfma_f32_16x16x32_bf16 v[12:15], v[60:63], v[96:99], v[12:15]\n"
    "s_addc_u32 s49, s49, 0\n"
    "s_waitcnt vmcnt(10) lgkmcnt(4)\n"
    "v_mfma_f32_16x16x32_bf16 v[0:3], v[16:19], v[100:103], v[0:3]\n"
    "ds_read_b128 v[48:51], v113 offset:2048\n"
    "ds_read_b128 v[52:55], v113 offset:2304\n"
    "v_mfma_f32_16x16x32_bf16 v[4:7], v[20:23], v[100:103], v[4:7]\n"
    "ds_read_b128 v[56:59], v113 offset:6144\n"
    "v_mfma_f32_16x16x32_bf16 v[8:11], v[24:27], v[100:103], v[8:11]\n"
    "ds_read_b128 v[60:63], v113 offset:6400\n"
    "v_mfma_f32_16x16x32_bf16 v[12:15], v[28:31], v[100:103], v[12:15]\n"
    "global_load_dwordx4 v[96:99], v118, s[48:49] offset:-4096 sc1\n"
    "s_cmp_ge_u32 s38, 18\n"
    "s_cbranch_scc0 .Ln_141\n"
    ".Lb_142:\n"
    "s_waitcnt vmcnt(10) lgkmcnt(4)\n"
    "v_mfma_f32_16x16x32_bf16 v[0:3], v[32:35], v[104:107], v[0:3]\n"
    "ds_read_b128 v[16:19], v113 offset:8192\n"
    "ds_read_b128 v[20:23], v113 offset:8448\n"
    "v_mfma_f32_16x16x32_bf16 v[4:7], v[36:39], v[104:107], v[4:7]\n"
    "ds_read_b128 v[24:27], v113 offset:12288\n"
    "ds_read_b128 v[28:31], v113 offset:12544\n"
    "v_mfma_f32_16x16x32_bf16 v[8:11], v[40:43], v[104:107], v[8:11]\n"
    "global_load_dwordx4 v[100:103], v118, s[48:49] offset:0 sc1\n"
    "s_add_u32 s48, s48, 0x2000\n"
    "v_mfma_f32_16x16x32_bf16 v[12:15], v[44:47], v[104:107], v[12:15]\n"
    "s_addc_u32 s49, s49, 0\n"
    "s_waitcnt vmcnt(10) lgkmcnt(4)\n"
    "v_mfma_f32_16x16x32_bf16 v[0:3], v[48:51], v[108:111], v[0:3]\n"
    "ds_read_b128 v[32:35], v113 offset:10240\n"
    "ds_read_b128 v[36:39], v113 offset:10496\n"
    "v_mfma_f32_16x16x32_bf16 v[4:7], v[52:55], v[108:111], v[4:7]\n"
    "ds_read_b128 v[40:43], v113 offset:14336\n"
    "v_mfma_f32_16x16x32_bf16 v[8:11], v[56:59], v[108:111], v[8:11]\n"
    "ds_read_b128 v[44:47], v113 offset:14592\n"
    "v_mfma_f32_16x16x32_bf16 v[12:15], v[60:63], v[108:111], v[12:15]\n"
    "global_load_dwordx4 v[104:107], v118, s[48:49] offset:-4096 sc1\n"
    "s_mov_b64 exec, 1\n"
    "v_mov_b32 v117, 1\n"
    "ds_add_u32 v119, v117 offset:176\n"
    "s_mov_b64 exec, -1\n"
    "s_waitcnt vmcnt(10) lgkmcnt(4)\n"
    "v_mfma_f32_16x16x32_bf16 v[0:3], v[16:19], v[64:67], v[0:3]\n"
    "ds_read_b128 v[48:51], v113 offset:16384\n"
    "ds_read_b128 v[52:55], v113 offset:16640\n"
    "v_mfma_f32_16x16x32_bf16 v[4:7], v[20:23], v[64:67], v[4:7]\n"
    "ds_read_b128 v[56:59], v113 offset:20480\n"
    "ds_read_b128 v[60:63], v113 offset:20736\n"
    "v_mfma_f32_16x16x32_bf16 v[8:11], v[24:27], v[64:67], v[8:11]\n"
    "global_load_dwordx4 v[108:111], v118, s[48:49] offset:0 sc1\n"
    "s_add_u32 s48, s48, 0x2000\n"
    "v_mfma_f32_16x16x32_bf16 v[12:15], v[28:31], v[64:67], v[12:15]\n"
    "s_addc_u32 s49, s49, 0\n"
    "ds_read_b32 v116, v119 offset:140\n"
    "s_waitcnt vmcnt(10) lgkmcnt(4)\n"
    "v_mfma_f32_16x16x32_bf16 v[0:3], v[32:35], v[68:71], v[0:3]\n"
    "ds_read_b128 v[16:19], v113 offset:18432\n"
    "ds_read_b128 v[20:23], v113 offset:18688\n"
    "v_mfma_f32_16x16x32_bf16 v[4:7], v[36:39], v[68:71], v[4:7]\n"
    "ds_read_b128 v[24:27], v113 offset:22528\n"
    "v_mfma_f32_16x16x32_bf16 v[8:11], v[40:43], v[68:71], v[8:11]\n"
    "ds_read_b128 v[28:31], v113 offset:22784\n"
    "v_mfma_f32_16x16x32_bf16 v[12:15], v[44:47], v[68:71], v[12:15]\n"
    "global_load_dwordx4 v[64:67], v118, s[48:49] offset:-4096 sc1\n"
    "s_waitcnt vmcnt(10) lgkmcnt(4)\n"
    "v_mfma_f32_16x16x32_bf16 v[0:3], v[48:51], v[72:75], v[0:3]\n"
    "ds_read_b128 v[32:35], v113 offset:24576\n"
    "ds_read_b128 v[36:39], v113 offset:24832\n"
    "v_mfma_f32_16x16x32_bf16 v[4:7], v[52:55], v[72:75], v[4:7]\n"
    "ds_read_b128 v[40:43], v113 offset:28672\n"
    "ds_read_b128 v[44:47], v113 offset:28928\n"
    "v_mfma_f32_16x16x32_bf16 v[8:11], v[56:59], v[72:75], v[8:11]\n"
    "global_load_dwordx4 v[68:71], v118, s[48:49] offset:0 sc1\n"
    "s_add_u32 s48, s48, 0x2000\n"
    "v_mfma_f32_16x16x32_bf16 v[12:15], v[60:63], v[72:75], v[12:15]\n"
    "s_addc_u32 s49, s49, 0\n"
    "v_readfirstlane_b32 s57, v116\n"
    "s_waitcnt vmcnt(10) lgkmcnt(4)\n"
    "v_mfma_f32_16x16x32_bf16 v[0:3], v[16:19], v[76:79], v[0:3]\n"
    "ds_read_b128 v[48:51], v113 offset:26624\n"
    "ds_read_b128 v[52:55], v113 offset:26880\n"
    "v_mfma_f32_16x16x32_bf16 v[4:7], v[20:23], v[76:79], v[4:7]\n"
    "ds_read_b128 v[56:59], v113 offset:30720\n"
    "v_mfma_f32_16x16x32_bf16 v[8:11], v[24:27], v[76:79], v[8:11]\n"
    "ds_read_b128 v[60:63], v113 offset:30976\n"
    "v_mfma_f32_16x16x32_bf16 v[12:15], v[28:31], v[76:79], v[12:15]\n"
    "global_load_dwordx4 v[72:75], v118, s[48:49] offset:-4096 sc1\n"
    "s_cmp_ge_u32 s38, 24\n"
    "s_cbranch_scc0 .Ln_143\n"
    ".Lb_144:\n"
    "s_waitcnt vmcnt(10) lgkmcnt(4)\n"
    "v_mfma_f32_16x16x32_bf16 v[0:3], v[32:35], v[80:83], v[0:3]\n"
    "ds_read_b128 v[16:19], v114 offset:0\n"
    "ds_read_b128 v[20:23], v114 offset:256\n"
    "v_mfma_f32_16x16x32_bf16 v[4:7], v[36:39], v[80:83], v[4:7]\n"
    "ds_read_b128 v[24:27], v114 offset:4096\n"
    "ds_read_b128 v[28:31], v114 offset:4352\n"
    "v_mfma_f32_16x16x32_bf16 v[8:11], v[40:43], v[80:83], v[8:11]\n"
    "global_load_dwordx4 v[76:79], v118, s[48:49] offset:0 sc1\n"
    "s_add_u32 s48, s48, 0x2000\n"
    "v_mfma_f32_16x16x32_bf16 v[12:15], v[44:47], v[80:83], v[12:15]\n"
    "s_addc_u32 s49, s49, 0\n"
    "s_cmp_eq_u32 s57, 0\n"
    "s_cbranch_scc1 .Lf_late\n"
    ".Lf_fast:\n"
    "s_waitcnt vmcnt(10) lgkmcnt(4)\n"
    "v_mfma_f32_16x16x32_bf16 v[0:3], v[48:51], v[84:87], v[0:3]\n"
    "ds_read_b128 v[32:35], v114 offset:2048\n"
    "ds_read_b128 v[36:39], v114 offset:2304\n"
    "v_mfma_f32_16x16x32_bf16 v[4:7], v[52:55], v[84:87], v[4:7]\n"
    "ds_read_b128 v[40:43], v114 offset:6144\n"
    "v_mfma_f32_16x16x32_bf16 v[8:11], v[56:59], v[84:87], v[8:11]\n"
    "ds_read_b128 v[44:47], v114 offset:6400\n"
    "v_mfma_f32_16x16x32_bf16 v[12:15], v[60:63], v[84:87], v[12:15]\n"
    "global_load_dwordx4 v[80:83], v118, s[50:51] offset:-4096 sc1\n"
    "s_waitcnt vmcnt(10) lgkmcnt(4)\n"
    "v_mfma_f32_16x16x32_bf16 v[0:3], v[16:19], v[88:91], v[0:3]\n"
    "ds_read_b128 v[48:51], v114 offset:8192\n"
    "ds_read_b128 v[52:55], v114 offset:8448\n"
    "v_mfma_f32_16x16x32_bf16 v[4:7], v[20:23], v[88:91], v[4:7]\n"
    "ds_read_b128 v[56:59], v114 offset:12288\n"
    "v_mfma_f32_16x16x32_bf16 v[8:11], v[24:27], v[88:91], v[8:11]\n"
    "ds_read_b128 v[60:63], v114 offset:12544\n"
    "v_mfma_f32_16x16x32_bf16 v[12:15], v[28:31], v[88:91], v[12:15]\n"
    "global_load_dwordx4 v[84:87], v118, s[50:51] offset:-2048 sc1\n"
    "s_waitcnt vmcnt(10) lgkmcnt(4)\n"
    "v_mfma_f32_16x16x32_bf16 v[0:3], v[32:35], v[92:95], v[0:3]\n"
    "ds_read_b128 v[16:19], v114 offset:10240\n"
    "ds_read_b128 v[20:23], v114 offset:10496\n"
    "v_mfma_f32_16x16x32_bf16 v[4:7], v[36:39], v[92:95], v[4:7]\n"
    "ds_read_b128 v[24:27], v114 offset:14336\n"
    "v_mfma_f32_16x16x32_bf16 v[8:11], v[40:43], v[92:95], v[8:11]\n"
    "ds_read_b128 v[28:31], v114 offset:14592\n"
    "v_mfma_f32_16x16x32_bf16 v[12:15], v[44:47], v[92:95], v[12:15]\n"
    "global_load_dwordx4 v[88:91], v118, s[50:51] offset:0 sc1\n"
    "s_waitcnt vmcnt(10) lgkmcnt(4)\n"
    "v_mfma_f32_16x16x32_bf16 v[0:3], v[48:51], v[96:99], v[0:3]\n"
    "ds_read_b128 v[32:35], v114 offset:16384\n"
    "ds_read_b128 v[36:39], v114 offset:16640\n"
    "v_mfma_f32_16x16x32_bf16 v[4:7], v[52:55], v[96:99], v[4:7]\n"
    "ds_read_b128 v[40:43], v114 offset:20480\n"
    "ds_read_b128 v[44:47], v114 offset:20736\n"
    "v_mfma_f32_16x16x32_bf16 v[8:11], v[56:59], v[96:99], v[8:11]\n"
    "global_load_dwordx4 v[92:95], v118, s[50:51] offset:2048 sc1\n"
    "s_add_u32 s50, s50, 0x2000\n"
    "v_mfma_f32_16x16x32_bf16 v[12:15], v[60:63], v[96:99], v[12:15]\n"
    "s_addc_u32 s51, s51, 0\n"
    "s_waitcnt vmcnt(10) lgkmcnt(4)\n"
    "v_mfma_f32_16x16x32_bf16 v[0:3], v[16:19], v[100:103], v[0:3]\n"
    "ds_read_b128 v[48:51], v114 offset:18432\n"
    "ds_read_b128 v[52:55], v114 offset:18688\n"
    "v_mfma_f32_16x16x32_bf16 v[4:7], v[20:23], v[100:103], v[4:7]\n"
    "ds_read_b128 v[56:59], v114 offset:22528\n"
    "v_mfma_f32_16x16x32_bf16 v[8:11], v[24:27], v[100:103], v[8:11]\n"
    "ds_read_b128 v[60:63], v114 offset:22784\n"
    "v_mfma_f32_16x16x32_bf16 v[12:15], v[28:31], v[100:103], v[12:15]\n"
    "global_load_dwordx4 v[96:99], v118, s[50:51] offset:-4096 sc1\n"
    "s_cmp_ge_u32 s38, 30\n"
    "s_cbranch_scc0 .Ln_145\n"
    ".Lb_146:\n"
    "s_waitcnt vmcnt(10) lgkmcnt(4)\n"
    "v_mfma_f32_16x16x32_bf16 v[0:3], v[32:35], v[104:107], v[0:3]\n"
    "ds_read_b128 v[16:19], v112 offset:0\n"
    "ds_read_b128 v[20:23], v112 offset:256\n"
    "v_mfma_f32_16x16x32_bf16 v[4:7], v[36:39], v[104:107], v[4:7]\n"
    "ds_read_b128 v[24:27], v112 offset:4096\n"
    "v_mfma_f32_16x16x32_bf16 v[8:11], v[40:43], v[104:107], v[8:11]\n"
    "ds_read_b128 v[28:31], v112 offset:4352\n"
    "v_mfma_f32_16x16x32_bf16 v[12:15], v[44:47], v[104:107], v[12:15]\n"
    "global_load_dwordx4 v[100:103], v118, s[50:51] offset:-2048 sc1\n"
    "s_waitcnt vmcnt(10) lgkmcnt(4)\n"
    "v_mfma_f32_16x16x32_bf16 v[0:3], v[48:51], v[108:111], v[0:3]\n"
    "ds_read_b128 v[32:35], v112 offset:2048\n"
    "ds_read_b128 v[36:39], v112 offset:2304\n"
    "v_mfma_f32_16x16x32_bf16 v[4:7], v[52:55], v[108:111], v[4:7]\n"
    "ds_read_b128 v[40:43], v112 offset:6144\n"
    "v_mfma_f32_16x16x32_bf16 v[8:11], v[56:59], v[108:111], v[8:11]\n"
    "ds_read_b128 v[44:47], v112 offset:6400\n"
    "v_mfma_f32_16x16x32_bf16 v[12:15], v[60:63], v[108:111], v[12:15]\n"
    "global_load_dwordx4 v[104:107], v118, s[50:51] offset:0 sc1\n"
    "s_waitcnt vmcnt(10) lgkmcnt(4)\n"
    "v_mfma_f32_16x16x32_bf16 v[0:3], v[16:19], v[64:67], v[0:3]\n"
    "ds_read_b128 v[48:51], v112 offset:8192\n"
    "ds_read_b128 v[52:55], v112 offset:8448\n"
    "v_mfma_f32_16x16x32_bf16 v[4:7], v[20:23], v[64:67], v[4:7]\n"
    "ds_read_b128 v[56:59], v112 offset:12288\n"
    "ds_read_b128 v[60:63], v112 offset:12544\n"
    "v_mfma_f32_16x16x32_bf16 v[8:11], v[24:27], v[64:67], v[8:11]\n"
    "global_load_dwordx4 v[108:111], v118, s[50:51] offset:2048 sc1\n"
    "s_add_u32 s50, s50, 0x2000\n"
    "v_mfma_f32_16x16x32_bf16 v[12:15], v[28:31], v[64:67], v[12:15]\n"
    "s_addc_u32 s51, s51, 0\n"
    "s_waitcnt vmcnt(10) lgkmcnt(4)\n"
    "v_mfma_f32_16x16x32_bf16 v[0:3], v[32:35], v[68:71], v[0:3]\n"
    "ds_read_b128 v[16:19], v112 offset:10240\n"
    "ds_read_b128 v[20:23], v112 offset:10496\n"
    "v_mfma_f32_16x16x32_bf16 v[4:7], v[36:39], v[68:71], v[4:7]\n"
    "ds_read_b128 v[24:27], v112 offset:14336\n"
    "v_mfma_f32_16x16x32_bf16 v[8:11], v[40:43], v[68:71], v[8:11]\n"
    "ds_read_b128 v[28:31], v112 offset:14592\n"
    "v_mfma_f32_16x16x32_bf16 v[12:15], v[44:47], v[68:71], v[12:15]\n"
    "global_load_dwordx4 v[64:67], v118, s[50:51] offset:-4096 sc1\n"
    "s_waitcnt vmcnt(10) lgkmcnt(4)\n"
    "v_mfma_f32_16x16x32_bf16 v[0:3], v[48:51], v[72:75], v[0:3]\n"
    "ds_read_b128 v[32:35], v112 offset:16384\n"
    "ds_read_b128 v[36:39], v112 offset:16640\n"
    "v_mfma_f32_16x16x32_bf16 v[4:7], v[52:55], v[72:75], v[4:7]\n"
    "ds_read_b128 v[40:43], v112 offset:20480\n"
    "v_mfma_f32_16x16x32_bf16 v[8:11], v[56:59], v[72:75], v[8:11]\n"
    "ds_read_b128 v[44:47], v112 offset:20736\n"
    "v_mfma_f32_16x16x32_bf16 v[12:15], v[60:63], v[72:75], v[12:15]\n"
    "global_load_dwordx4 v[68:71], v118, s[50:51] offset:-2048 sc1\n"
    "s_waitcnt vmcnt(9) lgkmcnt(4)\n"
    "v_mfma_f32_16x16x32_bf16 v[0:3], v[16:19], v[76:79], v[0:3]\n"
    "v_max3_u32 v117, v80, v81, v82\n"
    "v_max_u32 v117, v117, v83\n"
    "v_mfma_f32_16x16x32_bf16 v[4:7], v[20:23], v[76:79], v[4:7]\n"
    "v_cmp_eq_u32 vcc, -1, v117\n"
    "ds_read_b128 v[48:51], v112 offset:18432\n"
    "v_mfma_f32_16x16x32_bf16 v[8:11], v[24:27], v[76:79], v[8:11]\n"
    "ds_read_b128 v[52:55], v112 offset:18688\n"
    "ds_read_b128 v[56:59], v112 offset:22528\n"
    "v_mfma_f32_16x16x32_bf16 v[12:15], v[28:31], v[76:79], v[12:15]\n"
    "ds_read_b128 v[60:63], v112 offset:22784\n"
    "global_load_dwordx4 v[72:75], v118, s[50:51] offset:0 sc1\n"
    "s_cbranch_vccnz .Lx_147\n"
    ".Ly_148:\n"
    "s_cmp_ge_u32 s38, 38\n"
    "s_cbranch_scc0 .Ln_149\n"
    ".Lb_150:\n"
    "s_waitcnt vmcnt(9) lgkmcnt(4)\n"
    "v_mfma_f32_16x16x32_bf16 v[0:3], v[32:35], v[80:83], v[0:3]\n"
    "v_max3_u32 v117, v84, v85, v86\n"
    "v_max_u32 v117, v117, v87\n"
    "v_cmp_eq_u32 vcc, -1, v117\n"
    "v_mfma_f32_16x16x32_bf16 v[4:7], v[36:39], v[80:83], v[4:7]\n"
    "ds_read_b128 v[16:19], v113 offset:32768\n"
    "ds_read_b128 v[20:23], v113 offset:33024\n"
    "ds_read_b128 v[24:27], v113 offset:36864\n"
    "v_mfma_f32_16x16x32_bf16 v[8:11], v[40:43], v[80:83], v[8:11]\n"
    "ds_read_b128 v[28:31], v113 offset:37120\n"
    "global_load_dwordx4 v[76:79], v118, s[50:51] offset:2048 sc1\n"
    "v_mfma_f32_16x16x32_bf16 v[12:15], v[44:47], v[80:83], v[12:15]\n"
    "s_add_u32 s50, s50, 0x2000\n"
    "s_addc_u32 s51, s51, 0\n"
    "s_cbranch_vccnz .Lx_151\n"
    ".Ly_152:\n"
    ".Lf_join:\n"
    "s_waitcnt vmcnt(9) lgkmcnt(4)\n"
    "v_mfma_f32_16x16x32_bf16 v[0:3], v[48:51], v[84:87], v[0:3]\n"
    "v_max3_u32 v117, v88, v89, v90\n"
    "v_max_u32 v117, v117, v91\n"
    "v_mfma_f32_16x16x32_bf16 v[4:7], v[52:55], v[84:87], v[4:7]\n"
    "v_cmp_eq_u32 vcc, -1, v117\n"
    "ds_read_b128 v[32:35], v113 offset:34816\n"
    "v_mfma_f32_16x16x32_bf16 v[8:11], v[56:59], v[84:87], v[8:11]\n"
    "ds_read_b128 v[36:39], v113 offset:35072\n"
    "ds_read_b128 v[40:43], v113 offset:38912\n"
    "v_mfma_f32_16x16x32_bf16 v[12:15], v[60:63], v[84:87], v[12:15]\n"
    "ds_read_b128 v[44:47], v113 offset:39168\n"
    "global_load_dwordx4 v[80:83], v118, s[50:51] offset:-4096 sc1\n"
    "s_cbranch_vccnz .Lx_153\n"
    ".Ly_154:\n"
    "s_waitcnt vmcnt(9) lgkmcnt(4)\n"
    "v_mfma_f32_16x16x32_bf16 v[0:3], v[16:19], v[88:91], v[0:3]\n"
    "v_max3_u32 v117, v92, v93, v94\n"
    "v_max_u32 v117, v117, v95\n"
    "v_mfma_f32_16x16x32_bf16 v[4:7], v[20:23], v[88:91], v[4:7]\n"
    "v_cmp_eq_u32 vcc, -1, v117\n"
    "ds_read_b128 v[48:51], v113 offset:40960\n"
    "v_mfma_f32_16x16x32_bf16 v[8:11], v[24:27], v[88:91], v[8:11]\n"
    "ds_read_b128 v[52:55], v113 offset:41216\n"
    "ds_read_b128 v[56:59], v113 offset:45056\n"
    "v_mfma_f32_16x16x32_bf16 v[12:15], v[28:31], v[88:91], v[12:15]\n"
    "ds_read_b128 v[60:63], v113 offset:45312\n"
    "global_load_dwordx4 v[84:87], v118, s[50:51] offset:-2048 sc1\n"
    "s_cbranch_vccnz .Lx_155\n"
    ".Ly_156:\n"
    "s_waitcnt vmcnt(9) lgkmcnt(4)\n"
    "v_mfma_f32_16x16x32_bf16 v[0:3], v[32:35], v[92:95], v[0:3]\n"
    "v_max3_u32 v117, v96, v97, v98\n"
    "v_max_u32 v117, v117, v99\n"
    "v_mfma_f32_16x16x32_bf16 v[4:7], v[36:39], v[92:95], v[4:7]\n"
    "v_cmp_eq_u32 vcc, -1, v117\n"
    "ds_read_b128 v[16:19], v113 offset:43008\n"
    "v_mfma_f32_16x16x32_bf16 v[8:11], v[40:43], v[92:95], v[8:11]\n"
    "ds_read_b128 v[20:23], v113 offset:43264\n"
    "ds_read_b128 v[24:27], v113 offset:47104\n"
    "v_mfma_f32_16x16x32_bf16 v[12:15], v[44:47], v[92:95], v[12:15]\n"
    "ds_read_b128 v[28:31], v113 offset:47360\n"
    "global_load_dwordx4 v[88:91], v118, s[50:51] offset:0 sc1\n"
    "s_cbranch_vccnz .Lx_157\n"
    ".Ly_158:\n"
    "s_waitcnt vmcnt(9) lgkmcnt(4)\n"
    "v_mfma_f32_16x16x32_bf16 v[0:3], v[48:51], v[96:99], v[0:3]\n"
    "v_max3_u32 v117, v100, v101, v102\n"
    "v_max_u32 v117, v117, v103\n"
    "v_cmp_eq_u32 vcc, -1, v117\n"
    "v_mfma_f32_16x16x32_bf16 v[4:7], v[52:55], v[96:99], v[4:7]\n"
    "ds_read_b128 v[32:35], v113 offset:49152\n"
    "ds_read_b128 v[36:39], v113 offset:49408\n"
    "ds_read_b128 v[40:43], v113 offset:53248\n"
    "v_mfma_f32_16x16x32_bf16 v[8:11], v[56:59], v[96:99], v[8:11]\n"
    "ds_read_b128 v[44:47], v113 offset:53504\n"
    "global_load_dwordx4 v[92:95], v118, s[50:51] offset:2048 sc1\n"
    "v_mfma_f32_16x16x32_bf16 v[12:15], v[60:63], v[96:99], v[12:15]\n"
    "s_add_u32 s50, s50, 0x2000\n"
    "s_addc_u32 s51, s51, 0\n"
    "s_cbranch_vccnz .Lx_159\n"
    ".Ly_160:\n"
    "s_waitcnt vmcnt(9) lgkmcnt(4)\n"
    "v_mfma_f32_16x16x32_bf16 v[0:3], v[16:19], v[100:103], v[0:3]\n"
    "v_max3_u32 v117, v104, v105, v106\n"
    "v_max_u32 v117, v117, v107\n"
    "v_mfma_f32_16x16x32_bf16 v[4:7], v[20:23], v[100:103], v[4:7]\n"
    "v_cmp_eq_u32 vcc, -1, v117\n"
    "ds_read_b128 v[48:51], v113 offset:51200\n"
    "v_mfma_f32_16x16x32_bf16 v[8:11], v[24:27], v[100:103], v[8:11]\n"
    "ds_read_b128 v[52:55], v113 offset:51456\n"
    "ds_read_b128 v[56:59], v113 offset:55296\n"
    "v_mfma_f32_16x16x32_bf16 v[12:15], v[28:31], v[100:103], v[12:15]\n"
    "ds_read_b128 v[60:63], v113 offset:55552\n"
    "s_cbranch_vccnz .Lx_161\n"
    ".Ly_162:\n"
    "s_waitcnt vmcnt(8) lgkmcnt(4)\n"
    "v_mfma_f32_16x16x32_bf16 v[0:3], v[32:35], v[104:107], v[0:3]\n"
    "v_max3_u32 v117, v108, v109, v110\n"
    "v_max_u32 v117, v117, v111\n"
    "v_mfma_f32_16x16x32_bf16 v[4:7], v[36:39], v[104:107], v[4:7]\n"
    "v_cmp_eq_u32 vcc, -1, v117\n"
    "ds_read_b128 v[16:19], v113 offset:57344\n"
    "v_mfma_f32_16x16x32_bf16 v[8:11], v[40:43], v[104:107], v[8:11]\n"
    "ds_read_b128 v[20:23], v113 offset:57600\n"
    "ds_read_b128 v[24:27], v113 offset:61440\n"
    "v_mfma_f32_16x16x32_bf16 v[12:15], v[44:47], v[104:107], v[12:15]\n"
    "ds_read_b128 v[28:31], v113 offset:61696\n"
    "s_cbranch_vccnz .Lx_163\n"
    ".Ly_164:\n"
    "s_waitcnt vmcnt(7) lgkmcnt(4)\n"
    "v_mfma_f32_16x16x32_bf16 v[0:3], v[48:51], v[108:111], v[0:3]\n"
    "v_max3_u32 v117, v64, v65, v66\n"
    "v_max_u32 v117, v117, v67\n"
    "v_mfma_f32_16x16x32_bf16 v[4:7], v[52:55], v[108:111], v[4:7]\n"
    "v_cmp_eq_u32 vcc, -1, v117\n"
    "ds_read_b128 v[32:35], v113 offset:59392\n"
    "v_mfma_f32_16x16x32_bf16 v[8:11], v[56:59], v[108:111], v[8:11]\n"
    "ds_read_b128 v[36:39], v113 offset:59648\n"
    "ds_read_b128 v[40:43], v113 offset:63488\n"
    "v_mfma_f32_16x16x32_bf16 v[12:15], v[60:63], v[108:111], v[12:15]\n"
    "ds_read_b128 v[44:47], v113 offset:63744\n"
    "s_cbranch_vccnz .Lx_165\n"
    ".Ly_166:\n"
    "s_cmp_ge_u32 s38, 44\n"
    "s_cbranch_scc0 .Ln_167\n"
    ".Lb_168:\n"
    "s_waitcnt vmcnt(6) lgkmcnt(4)\n"
    "v_mfma_f32_16x16x32_bf16 v[0:3], v[16:19], v[64:67], v[0:3]\n"
    "v_max3_u32 v117, v68, v69, v70\n"
    "v_max_u32 v117, v117, v71\n"
    "v_mfma_f32_16x16x32_bf16 v[4:7], v[20:23], v[64:67], v[4:7]\n"
    "v_cmp_eq_u32 vcc, -1, v117\n"
    "ds_read_b128 v[48:51], v112 offset:24576\n"
    "v_mfma_f32_16x16x32_bf16 v[8:11], v[24:27], v[64:67], v[8:11]\n"
    "ds_read_b128 v[52:55], v112 offset:24832\n"
    "ds_read_b128 v[56:59], v112 offset:28672\n"
    "v_mfma_f32_16x16x32_bf16 v[12:15], v[28:31], v[64:67], v[12:15]\n"
    "ds_read_b128 v[60:63], v112 offset:28928\n"
    "s_cbranch_vccnz .Lx_169\n"
    ".Ly_170:\n"
    "s_waitcnt vmcnt(5) lgkmcnt(4)\n"
    "v_mfma_f32_16x16x32_bf16 v[0:3], v[32:35], v[68:71], v[0:3]\n"
    "v_max3_u32 v117, v72, v73, v74\n"
    "v_max_u32 v117, v117, v75\n"
    "v_mfma_f32_16x16x32_bf16 v[4:7], v[36:39], v[68:71], v[4:7]\n"
    "v_cmp_eq_u32 vcc, -1, v117\n"
    "ds_read_b128 v[16:19], v112 offset:26624\n"
    "v_mfma_f32_16x16x32_bf16 v[8:11], v[40:43], v[68:71], v[8:11]\n"
    "ds_read_b128 v[20:23], v112 offset:26880\n"
    "ds_read_b128 v[24:27], v112 offset:30720\n"
    "v_mfma_f32_16x16x32_bf16 v[12:15], v[44:47], v[68:71], v[12:15]\n"
    "ds_read_b128 v[28:31], v112 offset:30976\n"
    "s_cbranch_vccnz .Lx_171\n"
    ".Ly_172:\n"
    "s_waitcnt vmcnt(4) lgkmcnt(4)\n"
    "v_mfma_f32_16x16x32_bf16 v[0:3], v[48:51], v[72:75], v[0:3]\n"
    "v_max3_u32 v117, v76, v77, v78\n"
    "v_max_u32 v117, v117, v79\n"
    "v_mfma_f32_16x16x32_bf16 v[4:7], v[52:55], v[72:75], v[4:7]\n"
    "v_cmp_eq_u32 vcc, -1, v117\n"
    "ds_read_b128 v[32:35], v112 offset:32768\n"
    "v_mfma_f32_16x16x32_bf16 v[8:11], v[56:59], v[72:75], v[8:11]\n"
    "ds_read_b128 v[36:39], v112 offset:33024\n"
    "ds_read_b128 v[40:43], v112 offset:36864\n"
    "v_mfma_f32_16x16x32_bf16 v[12:15], v[60:63], v[72:75], v[12:15]\n"
    "ds_read_b128 v[44:47], v112 offset:37120\n"
    "s_cbranch_vccnz .Lx_173\n"
    ".Ly_174:\n"
    "s_waitcnt vmcnt(3) lgkmcnt(4)\n"
    "v_mfma_f32_16x16x32_bf16 v[0:3], v[16:19], v[76:79], v[0:3]\n"
    "v_max3_u32 v117, v80, v81, v82\n"
    "v_max_u32 v117, v117, v83\n"
    "v_mfma_f32_16x16x32_bf16 v[4:7], v[20:23], v[76:79], v[4:7]\n"
    "v_cmp_eq_u32 vcc, -1, v117\n"
    "ds_read_b128 v[48:51], v112 offset:34816\n"
    "v_mfma_f32_16x16x32_bf16 v[8:11], v[24:27], v[76:79], v[8:11]\n"
    "ds_read_b128 v[52:55], v112 offset:35072\n"
    "ds_read_b128 v[56:59], v112 offset:38912\n"
    "v_mfma_f32_16x16x32_bf16 v[12:15], v[28:31], v[76:79], v[12:15]\n"
    "ds_read_b128 v[60:63], v112 offset:39168\n"
    "s_cbranch_vccnz .Lx_175\n"
    ".Ly_176:\n"
    "s_waitcnt vmcnt(2) lgkmcnt(4)\n"
    "v_mfma_f32_16x16x32_bf16 v[0:3], v[32:35], v[80:83], v[0:3]\n"
    "v_max3_u32 v117, v84, v85, v86\n"
    "v_max_u32 v117, v117, v87\n"
    "v_mfma_f32_16x16x32_bf16 v[4:7], v[36:39], v[80:83], v[4:7]\n"
    "v_cmp_eq_u32 vcc, -1, v117\n"
    "ds_read_b128 v[16:19], v112 offset:40960\n"
    "v_mfma_f32_16x16x32_bf16 v[8:11], v[40:43], v[80:83], v[8:11]\n"
    "ds_read_b128 v[20:23], v112 offset:41216\n"
    "ds_read_b128 v[24:27], v112 offset:45056\n"
    "v_mfma_f32_16x16x32_bf16 v[12:15], v[44:47], v[80:83], v[12:15]\n"
    "ds_read_b128 v[28:31], v112 offset:45312\n"
    "s_cbranch_vccnz .Lx_177\n"
    ".Ly_178:\n"
    "s_waitcnt vmcnt(1) lgkmcnt(4)\n"
    "v_mfma_f32_16x16x32_bf16 v[0:3], v[48:51], v[84:87], v[0:3]\n"
    "v_max3_u32 v117, v88, v89, v90\n"
    "v_max_u32 v117, v117, v91\n"
    "v_mfma_f32_16x16x32_bf16 v[4:7], v[52:55], v[84:87], v[4:7]\n"
    "v_cmp_eq_u32 vcc, -1, v117\n"
    "ds_read_b128 v[32:35], v112 offset:43008\n"
    "v_mfma_f32_16x16x32_bf16 v[8:11], v[56:59], v[84:87], v[8:11]\n"
    "ds_read_b128 v[36:39], v112 offset:43264\n"
    "ds_read_b128 v[40:43], v112 offset:47104\n"
    "v_mfma_f32_16x16x32_bf16 v[12:15], v[60:63], v[84:87], v[12:15]\n"
    "ds_read_b128 v[44:47], v112 offset:47360\n"
    "s_cbranch_vccnz .Lx_179\n"
    ".Ly_180:\n"
    "s_waitcnt vmcnt(0) lgkmcnt(4)\n"
    "v_mfma_f32_16x16x32_bf16 v[0:3], v[16:19], v[88:91], v[0:3]\n"
    "v_max3_u32 v117, v92, v93, v94\n"
    "v_mfma_f32_16x16x32_bf16 v[4:7], v[20:23], v[88:91], v[4:7]\n"
    "v_max_u32 v117, v117, v95\n"
    "v_mfma_f32_16x16x32_bf16 v[8:11], v[24:27], v[88:91], v[8:11]\n"
    "v_cmp_eq_u32 vcc, -1, v117\n"
    "v_mfma_f32_16x16x32_bf16 v[12:15], v[28:31], v[88:91], v[12:15]\n"
    "s_cbranch_vccnz .Lx_181\n"
    ".Ly_182:\n"
    "s_waitcnt vmcnt(0) lgkmcnt(0)\n"
    "v_mfma_f32_16x16x32_bf16 v[0:3], v[32:35], v[92:95], v[0:3]\n"
    "v_mfma_f32_16x16x32_bf16 v[4:7], v[36:39], v[92:95], v[4:7]\n"
    "v_mfma_f32_16x16x32_bf16 v[8:11], v[40:43], v[92:95], v[8:11]\n"
    "v_mfma_f32_16x16x32_bf16 v[12:15], v[44:47], v[92:95], v[12:15]\n"
    "v_lshlrev_b32 v64, 16, v120\n"
    "v_and_b32 v65, 0xffff0000, v120\n"
    "v_lshlrev_b32 v66, 16, v121\n"
    "v_and_b32 v67, 0xffff0000, v121\n"
    "v_lshlrev_b32 v68, 16, v122\n"
    "v_and_b32 v69, 0xffff0000, v122\n"
    "v_lshlrev_b32 v70, 16, v123\n"
    "v_and_b32 v71, 0xffff0000, v123\n"
    "v_lshlrev_b32 v72, 16, v124\n"
    "v_and_b32 v73, 0xffff0000, v124\n"
    "v_lshlrev_b32 v74, 16, v125\n"
    "v_and_b32 v75, 0xffff0000, v125\n"
    "v_lshlrev_b32 v76, 16, v126\n"
    "v_and_b32 v77, 0xffff0000, v126\n"
    "v_lshlrev_b32 v78, 16, v127\n"
    "v_and_b32 v79, 0xffff0000, v127\n"
    "v_pk_add_f32 v[0:1], v[0:1], v[64:65]\n"
    "v_pk_add_f32 v[2:3], v[2:3], v[66:67]\n"
    "v_pk_add_f32 v[4:5], v[4:5], v[68:69]\n"
    "v_pk_add_f32 v[6:7], v[6:7], v[70:71]\n"
    "v_pk_add_f32 v[8:9], v[8:9], v[72:73]\n"
    "v_pk_add_f32 v[10:11], v[10:11], v[74:75]\n"
    "v_pk_add_f32 v[12:13], v[12:13], v[76:77]\n"
    "v_pk_add_f32 v[14:15], v[14:15], v[78:79]\n"
    "v_cvt_pk_bf16_f32 v16, v0, v1\n"
    "v_cvt_pk_bf16_f32 v17, v2, v3\n"
    "v_cvt_pk_bf16_f32 v18, v4, v5\n"
    "v_cvt_pk_bf16_f32 v19, v6, v7\n"
    "v_cvt_pk_bf16_f32 v20, v8, v9\n"
    "v_cvt_pk_bf16_f32 v21, v10, v11\n"
    "v_cvt_pk_bf16_f32 v22, v12, v13\n"
    "v_cvt_pk_bf16_f32 v23, v14, v15\n"
    "v_permlane16_swap_b32 v16, v18\n"
    "v_permlane16_swap_b32 v17, v19\n"
    "v_permlane16_swap_b32 v20, v22\n"
    "v_permlane16_swap_b32 v21, v23\n"
    "global_store_dwordx4 v115, v[16:19], s[46:47]\n"
    "global_store_dwordx4 v115, v[20:23], s[46:47] offset:64\n"
    "s_endpgm\n"
    ".Lf_late:\n"
    "s_setprio 3\n"
    ".Lw_183:\n"
    "ds_read_b32 v117, v119 offset:140\n"
    "s_waitcnt lgkmcnt(0)\n"
    "v_readfirstlane_b32 s60, v117\n"
    "s_cmp_ge_u32 s60, 1\n"
    "s_cbranch_scc1 .Ld_184\n"
    "s_sleep 1\n"
    "s_add_u32 s63, s63, 1\n"
    "s_bitcmp1_b32 s63, 19\n"
    "s_cbranch_scc0 .Lw_183\n"
    "s_sub_u32 s63, s63, 1\n"
    ".Ld_184:\n"
    "s_branch .Lf_fast\n"
    ".Lw_185:\n"
    "ds_read_b32 v117, v119 offset:120\n"
    "s_waitcnt lgkmcnt(0)\n"
    "v_readfirstlane_b32 s60, v117\n"
    "s_cmp_ge_u32 s60, 8\n"
    "s_cbranch_scc1 .Ld_186\n"
    "s_sleep 2\n"
    "s_add_u32 s63, s63, 1\n"
    "s_bitcmp1_b32 s63, 19\n"
    "s_cbranch_scc0 .Lw_185\n"
    "s_sub_u32 s63, s63, 1\n"
    ".Ld_186:\n"
    "s_waitcnt vmcnt(10) lgkmcnt(4)\n"
    "v_mfma_f32_16x16x32_bf16 v[0:3], v[48:51], v[84:87], v[0:3]\n"
    "ds_read_b128 v[32:35], v114 offset:2048\n"
    "v_mfma_f32_16x16x32_bf16 v[4:7], v[52:55], v[84:87], v[4:7]\n"
    "ds_read_b128 v[36:39], v114 offset:2304\n"
    "v_mfma_f32_16x16x32_bf16 v[8:11], v[56:59], v[84:87], v[8:11]\n"
    "ds_read_b128 v[40:43], v114 offset:6144\n"
    "v_mfma_f32_16x16x32_bf16 v[12:15], v[60:63], v[84:87], v[12:15]\n"
    "ds_read_b128 v[44:47], v114 offset:6400\n"
    "s_waitcnt vmcnt(9) lgkmcnt(4)\n"
    "v_mfma_f32_16x16x32_bf16 v[0:3], v[16:19], v[88:91], v[0:3]\n"
    "ds_read_b128 v[48:51], v114 offset:8192\n"
    "v_mfma_f32_16x16x32_bf16 v[4:7], v[20:23], v[88:91], v[4:7]\n"
    "ds_read_b128 v[52:55], v114 offset:8448\n"
    "v_mfma_f32_16x16x32_bf16 v[8:11], v[24:27], v[88:91], v[8:11]\n"
    "ds_read_b128 v[56:59], v114 offset:12288\n"
    "v_mfma_f32_16x16x32_bf16 v[12:15], v[28:31], v[88:91], v[12:15]\n"
    "ds_read_b128 v[60:63], v114 offset:12544\n"
    "s_waitcnt vmcnt(8) lgkmcnt(4)\n"
    "v_mfma_f32_16x16x32_bf16 v[0:3], v[32:35], v[92:95], v[0:3]\n"
    "ds_read_b128 v[16:19], v114 offset:10240\n"
    "v_mfma_f32_16x16x32_bf16 v[4:7], v[36:39], v[92:95], v[4:7]\n"
    "ds_read_b128 v[20:23], v114 offset:10496\n"
    "v_mfma_f32_16x16x32_bf16 v[8:11], v[40:43], v[92:95], v[8:11]\n"
    "ds_read_b128 v[24:27], v114 offset:14336\n"
    "v_mfma_f32_16x16x32_bf16 v[12:15], v[44:47], v[92:95], v[12:15]\n"
    "ds_read_b128 v[28:31], v114 offset:14592\n"
    "s_waitcnt vmcnt(7) lgkmcnt(4)\n"
    "v_mfma_f32_16x16x32_bf16 v[0:3], v[48:51], v[96:99], v[0:3]\n"
    "ds_read_b128 v[32:35], v114 offset:16384\n"
    "v_mfma_f32_16x16x32_bf16 v[4:7], v[52:55], v[96:99], v[4:7]\n"
    "ds_read_b128 v[36:39], v114 offset:16640\n"
    "v_mfma_f32_16x16x32_bf16 v[8:11], v[56:59], v[96:99], v[8:11]\n"
    "ds_read_b128 v[40:43], v114 offset:20480\n"
    "v_mfma_f32_16x16x32_bf16 v[12:15], v[60:63], v[96:99], v[12:15]\n"
    "ds_read_b128 v[44:47], v114 offset:20736\n"
    "s_waitcnt vmcnt(6) lgkmcnt(4)\n"
    "v_mfma_f32_16x16x32_bf16 v[0:3], v[16:19], v[100:103], v[0:3]\n"
    "ds_read_b128 v[48:51], v114 offset:18432\n"
    "v_mfma_f32_16x16x32_bf16 v[4:7], v[20:23], v[100:103], v[4:7]\n"
    "ds_read_b128 v[52:55], v114 offset:18688\n"
    "v_mfma_f32_16x16x32_bf16 v[8:11], v[24:27], v[100:103], v[8:11]\n"
    "ds_read_b128 v[56:59], v114 offset:22528\n"
    "v_mfma_f32_16x16x32_bf16 v[12:15], v[28:31], v[100:103], v[12:15]\n"
    "ds_read_b128 v[60:63], v114 offset:22784\n"
    "s_cmp_ge_u32 s38, 30\n"
    "s_cbranch_scc0 .Ln_187\n"
    ".Lb_188:\n"
    "s_waitcnt vmcnt(5) lgkmcnt(4)\n"
    "v_mfma_f32_16x16x32_bf16 v[0:3], v[32:35], v[104:107], v[0:3]\n"
    "ds_read_b128 v[16:19], v112 offset:0\n"
    "v_mfma_f32_16x16x32_bf16 v[4:7], v[36:39], v[104:107], v[4:7]\n"
    "ds_read_b128 v[20:23], v112 offset:256\n"
    "v_mfma_f32_16x16x32_bf16 v[8:11], v[40:43], v[104:107], v[8:11]\n"
    "ds_read_b128 v[24:27], v112 offset:4096\n"
    "v_mfma_f32_16x16x32_bf16 v[12:15], v[44:47], v[104:107], v[12:15]\n"
    "ds_read_b128 v[28:31], v112 offset:4352\n"
    "s_waitcnt vmcnt(4) lgkmcnt(4)\n"
    "v_mfma_f32_16x16x32_bf16 v[0:3], v[48:51], v[108:111], v[0:3]\n"
    "ds_read_b128 v[32:35], v112 offset:2048\n"
    "v_mfma_f32_16x16x32_bf16 v[4:7], v[52:55], v[108:111], v[4:7]\n"
    "ds_read_b128 v[36:39], v112 offset:2304\n"
    "v_mfma_f32_16x16x32_bf16 v[8:11], v[56:59], v[108:111], v[8:11]\n"
    "ds_read_b128 v[40:43], v112 offset:6144\n"
    "v_mfma_f32_16x16x32_bf16 v[12:15], v[60:63], v[108:111], v[12:15]\n"
    "ds_read_b128 v[44:47], v112 offset:6400\n"
    "s_waitcnt vmcnt(3) lgkmcnt(4)\n"
    "v_mfma_f32_16x16x32_bf16 v[0:3], v[16:19], v[64:67], v[0:3]\n"
    "ds_read_b128 v[48:51], v112 offset:8192\n"
    "v_mfma_f32_16x16x32_bf16 v[4:7], v[20:23], v[64:67], v[4:7]\n"
    "ds_read_b128 v[52:55], v112 offset:8448\n"
    "v_mfma_f32_16x16x32_bf16 v[8:11], v[24:27], v[64:67], v[8:11]\n"
    "ds_read_b128 v[56:59], v112 offset:12288\n"
    "v_mfma_f32_16x16x32_bf16 v[12:15], v[28:31], v[64:67], v[12:15]\n"
    "ds_read_b128 v[60:63], v112 offset:12544\n"
    "s_waitcnt vmcnt(2) lgkmcnt(4)\n"
    "v_mfma_f32_16x16x32_bf16 v[0:3], v[32:35], v[68:71], v[0:3]\n"
    "ds_read_b128 v[16:19], v112 offset:10240\n"
    "v_mfma_f32_16x16x32_bf16 v[4:7], v[36:39], v[68:71], v[4:7]\n"
    "ds_read_b128 v[20:23], v112 offset:10496\n"
    "v_mfma_f32_16x16x32_bf16 v[8:11], v[40:43], v[68:71], v[8:11]\n"
    "ds_read_b128 v[24:27], v112 offset:14336\n"
    "v_mfma_f32_16x16x32_bf16 v[12:15], v[44:47], v[68:71], v[12:15]\n"
    "ds_read_b128 v[28:31], v112 offset:14592\n"
    "s_waitcnt vmcnt(1) lgkmcnt(4)\n"
    "v_mfma_f32_16x16x32_bf16 v[0:3], v[48:51], v[72:75], v[0:3]\n"
    "ds_read_b128 v[32:35], v112 offset:16384\n"
    "v_mfma_f32_16x16x32_bf16 v[4:7], v[52:55], v[72:75], v[4:7]\n"
    "ds_read_b128 v[36:39], v112 offset:16640\n"
    "v_mfma_f32_16x16x32_bf16 v[8:11], v[56:59], v[72:75], v[8:11]\n"
    "ds_read_b128 v[40:43], v112 offset:20480\n"
    "v_mfma_f32_16x16x32_bf16 v[12:15], v[60:63], v[72:75], v[12:15]\n"
    "ds_read_b128 v[44:47], v112 offset:20736\n"
    "s_waitcnt vmcnt(0) lgkmcnt(4)\n"
    "v_mfma_f32_16x16x32_bf16 v[0:3], v[16:19], v[76:79], v[0:3]\n"
    "ds_read_b128 v[48:51], v112 offset:18432\n"
    "v_mfma_f32_16x16x32_bf16 v[4:7], v[20:23], v[76:79], v[4:7]\n"
    "ds_read_b128 v[52:55], v112 offset:18688\n"
    "v_mfma_f32_16x16x32_bf16 v[8:11], v[24:27], v[76:79], v[8:11]\n"
    "ds_read_b128 v[56:59], v112 offset:22528\n"
    "v_mfma_f32_16x16x32_bf16 v[12:15], v[28:31], v[76:79], v[12:15]\n"
    "ds_read_b128 v[60:63], v112 offset:22784\n"
    "s_setprio 3\n"
    "ds_read_b64 v[116:117], v119 offset:160\n"
    "s_waitcnt lgkmcnt(0)\n"
    "v_min_u32 v117, v116, v117\n"
    "s_nop 0\n"
    "v_readfirstlane_b32 s38, v117\n"
    ".Lw_189:\n"
    "ds_read_b32 v117, v119 offset:140\n"
    "s_waitcnt lgkmcnt(0)\n"
    "v_readfirstlane_b32 s60, v117\n"
    "s_cmp_ge_u32 s60, 1\n"
    "s_cbranch_scc1 .Ld_190\n"
    "s_sleep 1\n"
    "s_add_u32 s63, s63, 1\n"
    "s_bitcmp1_b32 s63, 19\n"
    "s_cbranch_scc0 .Lw_189\n"
    "s_sub_u32 s63, s63, 1\n"
    ".Ld_190:\n"
    ".Lf_o:\n"
    "global_load_dwordx4 v[80:83], v118, s[50:51] offset:-4096 sc1\n"
    "global_load_dwordx4 v[84:87], v118, s[50:51] offset:-2048 sc1\n"
    "global_load_dwordx4 v[88:91], v118, s[50:51] offset:0 sc1\n"
    "global_load_dwordx4 v[92:95], v118, s[50:51] offset:2048 sc1\n"
    "s_add_u32 s50, s50, 0x2000\n"
    "s_addc_u32 s51, s51, 0\n"
    "global_load_dwordx4 v[96:99], v118, s[50:51] offset:-4096 sc1\n"
    "global_load_dwordx4 v[100:103], v118, s[50:51] offset:-2048 sc1\n"
    "global_load_dwordx4 v[104:107], v118, s[50:51] offset:0 sc1\n"
    "global_load_dwordx4 v[108:111], v118, s[50:51] offset:2048 sc1\n"
    "s_add_u32 s50, s50, 0x2000\n"
    "s_addc_u32 s51, s51, 0\n"
    "global_load_dwordx4 v[64:67], v118, s[50:51] offset:-4096 sc1\n"
    "global_load_dwordx4 v[68:71], v118, s[50:51] offset:-2048 sc1\n"
    "global_load_dwordx4 v[72:75], v118, s[50:51] offset:0 sc1\n"
    "s_cmp_ge_u32 s38, 38\n"
    "s_cbranch_scc0 .Ln_191\n"
    ".Lb_192:\n"
    "s_waitcnt vmcnt(10)\n"
    "v_max3_u32 v117, v80, v81, v82\n"
    "v_max_u32 v117, v117, v83\n"
    "v_cmp_eq_u32 vcc, -1, v117\n"
    "s_nop 4\n"
    "s_cbranch_vccnz .Lx_193\n"
    ".Ly_194:\n"
    "s_waitcnt vmcnt(9) lgkmcnt(4)\n"
    "v_mfma_f32_16x16x32_bf16 v[0:3], v[32:35], v[80:83], v[0:3]\n"
    "v_max3_u32 v117, v84, v85, v86\n"
    "v_max_u32 v117, v117, v87\n"
    "v_cmp_eq_u32 vcc, -1, v117\n"
    "v_mfma_f32_16x16x32_bf16 v[4:7], v[36:39], v[80:83], v[4:7]\n"
    "ds_read_b128 v[16:19], v113 offset:32768\n"
    "ds_read_b128 v[20:23], v113 offset:33024\n"
    "ds_read_b128 v[24:27], v113 offset:36864\n"
    "v_mfma_f32_16x16x32_bf16 v[8:11], v[40:43], v[80:83], v[8:11]\n"
    "ds_read_b128 v[28:31], v113 offset:37120\n"
    "global_load_dwordx4 v[76:79], v118, s[50:51] offset:2048 sc1\n"
    "v_mfma_f32_16x16x32_bf16 v[12:15], v[44:47], v[80:83], v[12:15]\n"
    "s_add_u32 s50, s50, 0x2000\n"
    "s_addc_u32 s51, s51, 0\n"
    "s_cbranch_vccnz .Lx_195\n"
    ".Ly_196:\n"
    "s_branch .Lf_join\n"
    ".Lx_147:\n"
    "s_add_u32 s42, s58, 0\n"
    "s_addc_u32 s43, s59, 0\n"
    "s_add_u32 s37, s37, 1\n"
    "global_load_dwordx4 v[80:83], v118, s[42:43] offset:-4096 sc1\n"
    "s_cmp_lt_u32 s37, 32768\n"
    "s_waitcnt vmcnt(0)\n"
    "v_max3_u32 v117, v80, v81, v82\n"
    "v_max_u32 v117, v117, v83\n"
    "v_cmp_eq_u32 vcc, -1, v117\n"
    "s_cselect_b64 vcc, vcc, 0\n"
    "s_cmp_lg_u64 vcc, 0\n"
    "s_cbranch_scc1 .Lx_147\n"
    "s_branch .Ly_148\n"
    ".Lx_151:\n"
    "s_add_u32 s42, s58, 2048\n"
    "s_addc_u32 s43, s59, 0\n"
    "s_add_u32 s37, s37, 1\n"
    "global_load_dwordx4 v[84:87], v118, s[42:43] offset:-4096 sc1\n"
    "s_cmp_lt_u32 s37, 32768\n"
    "s_waitcnt vmcnt(0)\n"
    "v_max3_u32 v117, v84, v85, v86\n"
    "v_max_u32 v117, v117, v87\n"
    "v_cmp_eq_u32 vcc, -1, v117\n"
    "s_cselect_b64 vcc, vcc, 0\n"
    "s_cmp_lg_u64 vcc, 0\n"
    "s_cbranch_scc1 .Lx_151\n"
    "s_branch .Ly_152\n"
    ".Lx_153:\n"
    "s_add_u32 s42, s58, 4096\n"
    "s_addc_u32 s43, s59, 0\n"
    "s_add_u32 s37, s37, 1\n"
    "global_load_dwordx4 v[88:91], v118, s[42:43] offset:-4096 sc1\n"
    "s_cmp_lt_u32 s37, 32768\n"
    "s_waitcnt vmcnt(0)\n"
    "v_max3_u32 v117, v88, v89, v90\n"
    "v_max_u32 v117, v117, v91\n"
    "v_cmp_eq_u32 vcc, -1, v117\n"
    "s_cselect_b64 vcc, vcc, 0\n"
    "s_cmp_lg_u64 vcc, 0\n"
    "s_cbranch_scc1 .Lx_153\n"
    "s_branch .Ly_154\n"
    ".Lx_155:\n"
    "s_add_u32 s42, s58, 6144\n"
    "s_addc_u32 s43, s59, 0\n"
    "s_add_u32 s37, s37, 1\n"
    "global_load_dwordx4 v[92:95], v118, s[42:43] offset:-4096 sc1\n"
    "s_cmp_lt_u32 s37, 32768\n"
    "s_waitcnt vmcnt(0)\n"
    "v_max3_u32 v117, v92, v93, v94\n"
    "v_max_u32 v117, v117, v95\n"
    "v_cmp_eq_u32 vcc, -1, v117\n"
    "s_cselect_b64 vcc, vcc, 0\n"
    "s_cmp_lg_u64 vcc, 0\n"
    "s_cbranch_scc1 .Lx_155\n"
    "s_branch .Ly_156\n"
    ".Lx_157:\n"
    "s_add_u32 s42, s58, 8192\n"
    "s_addc_u32 s43, s59, 0\n"
    "s_add_u32 s37, s37, 1\n"
    "global_load_dwordx4 v[96:99], v118, s[42:43] offset:-4096 sc1\n"
    "s_cmp_lt_u32 s37, 32768\n"
    "s_waitcnt vmcnt(0)\n"
    "v_max3_u32 v117, v96, v97, v98\n"
    "v_max_u32 v117, v117, v99\n"
    "v_cmp_eq_u32 vcc, -1, v117\n"
    "s_cselect_b64 vcc, vcc, 0\n"
    "s_cmp_lg_u64 vcc, 0\n"
    "s_cbranch_scc1 .Lx_157\n"
    "s_branch .Ly_158\n"
    ".Lx_159:\n"
    "s_add_u32 s42, s58, 10240\n"
    "s_addc_u32 s43, s59, 0\n"
    "s_add_u32 s37, s37, 1\n"
    "global_load_dwordx4 v[100:103], v118, s[42:43] offset:-4096 sc1\n"
    "s_cmp_lt_u32 s37, 32768\n"
    "s_waitcnt vmcnt(0)\n"
    "v_max3_u32 v117, v100, v101, v102\n"
    "v_max_u32 v117, v117, v103\n"
    "v_cmp_eq_u32 vcc, -1, v117\n"
    "s_cselect_b64 vcc, vcc, 0\n"
    "s_cmp_lg_u64 vcc, 0\n"
    "s_cbranch_scc1 .Lx_159\n"
    "s_branch .Ly_160\n"
    ".Lx_161:\n"
    "s_add_u32 s42, s58, 12288\n"
    "s_addc_u32 s43, s59, 0\n"
    "s_add_u32 s37, s37, 1\n"
    "global_load_dwordx4 v[104:107], v118, s[42:43] offset:-4096 sc1\n"
    "s_cmp_lt_u32 s37, 32768\n"
    "s_waitcnt vmcnt(0)\n"
    "v_max3_u32 v117, v104, v105, v106\n"
    "v_max_u32 v117, v117, v107\n"
    "v_cmp_eq_u32 vcc, -1, v117\n"
    "s_cselect_b64 vcc, vcc, 0\n"
    "s_cmp_lg_u64 vcc, 0\n"
    "s_cbranch_scc1 .Lx_161\n"
    "s_branch .Ly_162\n"
    ".Lx_163:\n"
    "s_add_u32 s42, s58, 14336\n"
    "s_addc_u32 s43, s59, 0\n"
    "s_add_u32 s37, s37, 1\n"
    "global_load_dwordx4 v[108:111], v118, s[42:43] offset:-4096 sc1\n"
    "s_cmp_lt_u32 s37, 32768\n"
    "s_waitcnt vmcnt(0)\n"
    "v_max3_u32 v117, v108, v109, v110\n"
    "v_max_u32 v117, v117, v111\n"
    "v_cmp_eq_u32 vcc, -1, v117\n"
    "s_cselect_b64 vcc, vcc, 0\n"
    "s_cmp_lg_u64 vcc, 0\n"
    "s_cbranch_scc1 .Lx_163\n"
    "s_branch .Ly_164\n"
    ".Lx_165:\n"
    "s_add_u32 s42, s58, 16384\n"
    "s_addc_u32 s43, s59, 0\n"
    "s_add_u32 s37, s37, 1\n"
    "global_load_dwordx4 v[64:67], v118, s[42:43] offset:-4096 sc1\n"
    "s_cmp_lt_u32 s37, 32768\n"
    "s_waitcnt vmcnt(0)\n"
    "v_max3_u32 v117, v64, v65, v66\n"
    "v_max_u32 v117, v117, v67\n"
    "v_cmp_eq_u32 vcc, -1, v117\n"
    "s_cselect_b64 vcc, vcc, 0\n"
    "s_cmp_lg_u64 vcc, 0\n"
    "s_cbranch_scc1 .Lx_165\n"
    "s_branch .Ly_166\n"
    ".Lx_169:\n"
    "s_add_u32 s42, s58, 18432\n"
    "s_addc_u32 s43, s59, 0\n"
    "s_add_u32 s37, s37, 1\n"
    "global_load_dwordx4 v[68:71], v118, s[42:43] offset:-4096 sc1\n"
    "s_cmp_lt_u32 s37, 32768\n"
    "s_waitcnt vmcnt(0)\n"
    "v_max3_u32 v117, v68, v69, v70\n"
    "v_max_u32 v117, v117, v71\n"
    "v_cmp_eq_u32 vcc, -1, v117\n"
    "s_cselect_b64 vcc, vcc, 0\n"
    "s_cmp_lg_u64 vcc, 0\n"
    "s_cbranch_scc1 .Lx_169\n"
    "s_branch .Ly_170\n"
    ".Lx_171:\n"
    "s_add_u32 s42, s58, 20480\n"
    "s_addc_u32 s43, s59, 0\n"
    "s_add_u32 s37, s37, 1\n"
    "global_load_dwordx4 v[72:75], v118, s[42:43] offset:-4096 sc1\n"
    "s_cmp_lt_u32 s37, 32768\n"
    "s_waitcnt vmcnt(0)\n"
    "v_max3_u32 v117, v72, v73, v74\n"
    "v_max_u32 v117, v117, v75\n"
    "v_cmp_eq_u32 vcc, -1, v117\n"
    "s_cselect_b64 vcc, vcc, 0\n"
    "s_cmp_lg_u64 vcc, 0\n"
    "s_cbranch_scc1 .Lx_171\n"
    "s_branch .Ly_172\n"
    ".Lx_173:\n"
    "s_add_u32 s42, s58, 22528\n"
    "s_addc_u32 s43, s59, 0\n"
    "s_add_u32 s37, s37, 1\n"
    "global_load_dwordx4 v[76:79], v118, s[42:43] offset:-4096 sc1\n"
    "s_cmp_lt_u32 s37, 32768\n"
    "s_waitcnt vmcnt(0)\n"
    "v_max3_u32 v117, v76, v77, v78\n"
    "v_max_u32 v117, v117, v79\n"
    "v_cmp_eq_u32 vcc, -1, v117\n"
    "s_cselect_b64 vcc, vcc, 0\n"
    "s_cmp_lg_u64 vcc, 0\n"
    "s_cbranch_scc1 .Lx_173\n"
    "s_branch .Ly_174\n"
    ".Lx_175:\n"
    "s_add_u32 s42, s58, 24576\n"
    "s_addc_u32 s43, s59, 0\n"
    "s_add_u32 s37, s37, 1\n"
    "global_load_dwordx4 v[80:83], v118, s[42:43] offset:-4096 sc1\n"
    "s_cmp_lt_u32 s37, 32768\n"
    "s_waitcnt vmcnt(0)\n"
    "v_max3_u32 v117, v80, v81, v82\n"
    "v_max_u32 v117, v117, v83\n"
    "v_cmp_eq_u32 vcc, -1, v117\n"
    "s_cselect_b64 vcc, vcc, 0\n"
    "s_cmp_lg_u64 vcc, 0\n"
    "s_cbranch_scc1 .Lx_175\n"
    "s_branch .Ly_176\n"
    ".Lx_177:\n"
    "s_add_u32 s42, s58, 26624\n"
    "s_addc_u32 s43, s59, 0\n"
    "s_add_u32 s37, s37, 1\n"
    "global_load_dwordx4 v[84:87], v118, s[42:43] offset:-4096 sc1\n"
    "s_cmp_lt_u32 s37, 32768\n"
    "s_waitcnt vmcnt(0)\n"
    "v_max3_u32 v117, v84, v85, v86\n"
    "v_max_u32 v117, v117, v87\n"
    "v_cmp_eq_u32 vcc, -1, v117\n"
    "s_cselect_b64 vcc, vcc, 0\n"
    "s_cmp_lg_u64 vcc, 0\n"
    "s_cbranch_scc1 .Lx_177\n"
    "s_branch .Ly_178\n"
    ".Lx_179:\n"
    "s_add_u32 s42, s58, 28672\n"
    "s_addc_u32 s43, s59, 0\n"
    "s_add_u32 s37, s37, 1\n"
    "global_load_dwordx4 v[88:91], v118, s[42:43] offset:-4096 sc1\n"
    "s_cmp_lt_u32 s37, 32768\n"
    "s_waitcnt vmcnt(0)\n"
    "v_max3_u32 v117, v88, v89, v90\n"
    "v_max_u32 v117, v117, v91\n"
    "v_cmp_eq_u32 vcc, -1, v117\n"
    "s_cselect_b64 vcc, vcc, 0\n"
    "s_cmp_lg_u64 vcc, 0\n"
    "s_cbranch_scc1 .Lx_179\n"
    "s_branch .Ly_180\n"
    ".Lx_181:\n"
    "s_add_u32 s42, s58, 30720\n"
    "s_addc_u32 s43, s59, 0\n"
    "s_add_u32 s37, s37, 1\n"
    "global_load_dwordx4 v[92:95], v118, s[42:43] offset:-4096 sc1\n"
    "s_cmp_lt_u32 s37, 32768\n"
    "s_waitcnt vmcnt(0)\n"
    "v_max3_u32 v117, v92, v93, v94\n"
    "v_max_u32 v117, v117, v95\n"
    "v_cmp_eq_u32 vcc, -1, v117\n"
    "s_cselect_b64 vcc, vcc, 0\n"
    "s_cmp_lg_u64 vcc, 0\n"
    "s_cbranch_scc1 .Lx_181\n"
    "s_branch .Ly_182\n"
    ".Lx_193:\n"
    "s_add_u32 s42, s58, 0\n"
    "s_addc_u32 s43, s59, 0\n"
    "s_add_u32 s37, s37, 1\n"
    "global_load_dwordx4 v[80:83], v118, s[42:43] offset:-4096 sc1\n"
    "s_cmp_lt_u32 s37, 32768\n"
    "s_waitcnt vmcnt(0)\n"
    "v_max3_u32 v117, v80, v81, v82\n"
    "v_max_u32 v117, v117, v83\n"
    "v_cmp_eq_u32 vcc, -1, v117\n"
    "s_cselect_b64 vcc, vcc, 0\n"
    "s_cmp_lg_u64 vcc, 0\n"
    "s_cbranch_scc1 .Lx_193\n"
    "s_branch .Ly_194\n"
    ".Lx_195:\n"
    "s_add_u32 s42, s58, 2048\n"
    "s_addc_u32 s43, s59, 0\n"
    "s_add_u32 s37, s37, 1\n"
    "global_load_dwordx4 v[84:87], v118, s[42:43] offset:-4096 sc1\n"
    "s_cmp_lt_u32 s37, 32768\n"
    "s_waitcnt vmcnt(0)\n"
    "v_max3_u32 v117, v84, v85, v86\n"
    "v_max_u32 v117, v117, v87\n"
    "v_cmp_eq_u32 vcc, -1, v117\n"
    "s_cselect_b64 vcc, vcc, 0\n"
    "s_cmp_lg_u64 vcc, 0\n"
    "s_cbranch_scc1 .Lx_195\n"
    "s_branch .Ly_196\n"
    ".Ln_135:\n"
    ".Lw_197:\n"
    "ds_read_b64 v[116:117], v119 offset:160\n"
    "s_waitcnt lgkmcnt(0)\n"
    "v_min_u32 v117, v116, v117\n"
    "s_nop 0\n"
    "v_readfirstlane_b32 s38, v117\n"
    "s_cmp_ge_u32 s38, 6\n"
    "s_cbranch_scc1 .Ld_198\n"
    "s_sleep 1\n"
    "s_add_u32 s63, s63, 1\n"
    "s_bitcmp1_b32 s63, 19\n"
    "s_cbranch_scc0 .Lw_197\n"
    "s_sub_u32 s63, s63, 1\n"
    ".Ld_198:\n"
    "s_branch .Lb_136\n"
    ".Ln_139:\n"
    ".Lw_199:\n"
    "ds_read_b64 v[116:117], v119 offset:160\n"
    "s_waitcnt lgkmcnt(0)\n"
    "v_min_u32 v117, v116, v117\n"
    "s_nop 0\n"
    "v_readfirstlane_b32 s38, v117\n"
    "s_cmp_ge_u32 s38, 12\n"
    "s_cbranch_scc1 .Ld_200\n"
    "s_sleep 1\n"
    "s_add_u32 s63, s63, 1\n"
    "s_bitcmp1_b32 s63, 19\n"
    "s_cbranch_scc0 .Lw_199\n"
    "s_sub_u32 s63, s63, 1\n"
    ".Ld_200:\n"
    "s_branch .Lb_140\n"
    ".Ln_141:\n"
    ".Lw_201:\n"
    "ds_read_b64 v[116:117], v119 offset:160\n"
    "s_waitcnt lgkmcnt(0)\n"
    "v_min_u32 v117, v116, v117\n"
    "s_nop 0\n"
    "v_readfirstlane_b32 s38, v117\n"
    "s_cmp_ge_u32 s38, 18\n"
    "s_cbranch_scc1 .Ld_202\n"
    "s_sleep 1\n"
    "s_add_u32 s63, s63, 1\n"
    "s_bitcmp1_b32 s63, 19\n"
    "s_cbranch_scc0 .Lw_201\n"
    "s_sub_u32 s63, s63, 1\n"
    ".Ld_202:\n"
    "s_branch .Lb_142\n"
    ".Ln_143:\n"
    ".Lw_203:\n"
    "ds_read_b64 v[116:117], v119 offset:160\n"
    "s_waitcnt lgkmcnt(0)\n"
    "v_min_u32 v117, v116, v117\n"
    "s_nop 0\n"
    "v_readfirstlane_b32 s38, v117\n"
    "s_cmp_ge_u32 s38, 24\n"
    "s_cbranch_scc1 .Ld_204\n"
    "s_sleep 1\n"
    "s_add_u32 s63, s63, 1\n"
    "s_bitcmp1_b32 s63, 19\n"
    "s_cbranch_scc0 .Lw_203\n"
    "s_sub_u32 s63, s63, 1\n"
    ".Ld_204:\n"
    "s_branch .Lb_144\n"
    ".Ln_145:\n"
    ".Lw_205:\n"
    "ds_read_b64 v[116:117], v119 offset:160\n"
    "s_waitcnt lgkmcnt(0)\n"
    "v_min_u32 v117, v116, v117\n"
    "s_nop 0\n"
    "v_readfirstlane_b32 s38, v117\n"
    "s_cmp_ge_u32 s38, 30\n"
    "s_cbranch_scc1 .Ld_206\n"
    "s_sleep 1\n"
    "s_add_u32 s63, s63, 1\n"
    "s_bitcmp1_b32 s63, 19\n"
    "s_cbranch_scc0 .Lw_205\n"
    "s_sub_u32 s63, s63, 1\n"
    ".Ld_206:\n"
    "s_branch .Lb_146\n"
    ".Ln_149:\n"
    ".Lw_207:\n"
    "ds_read_b64 v[116:117], v119 offset:160\n"
    "s_waitcnt lgkmcnt(0)\n"
    "v_min_u32 v117, v116, v117\n"
    "s_nop 0\n"
    "v_readfirstlane_b32 s38, v117\n"
    "s_cmp_ge_u32 s38, 38\n"
    "s_cbranch_scc1 .Ld_208\n"
    "s_sleep 1\n"
    "s_add_u32 s63, s63, 1\n"
    "s_bitcmp1_b32 s63, 19\n"
    "s_cbranch_scc0 .Lw_207\n"
    "s_sub_u32 s63, s63, 1\n"
    ".Ld_208:\n"
    "s_branch .Lb_150\n"
    ".Ln_167:\n"
    ".Lw_209:\n"
    "ds_read_b64 v[116:117], v119 offset:160\n"
    "s_waitcnt lgkmcnt(0)\n"
    "v_min_u32 v117, v116, v117\n"
    "s_nop 0\n"
    "v_readfirstlane_b32 s38, v117\n"
    "s_cmp_ge_u32 s38, 44\n"
    "s_cbranch_scc1 .Ld_210\n"
    "s_sleep 1\n"
    "s_add_u32 s63, s63, 1\n"
    "s_bitcmp1_b32 s63, 19\n"
    "s_cbranch_scc0 .Lw_209\n"
    "s_sub_u32 s63, s63, 1\n"
    ".Ld_210:\n"
    "s_branch .Lb_168\n"
    ".Ln_187:\n"
    ".Lw_211:\n"
    "ds_read_b64 v[116:117], v119 offset:160\n"
    "s_waitcnt lgkmcnt(0)\n"
    "v_min_u32 v117, v116, v117\n"
    "s_nop 0\n"
    "v_readfirstlane_b32 s38, v117\n"
    "s_cmp_ge_u32 s38, 30\n"
    "s_cbranch_scc1 .Ld_212\n"
    "s_sleep 1\n"
    "s_add_u32 s63, s63, 1\n"
    "s_bitcmp1_b32 s63, 19\n"
    "s_cbranch_scc0 .Lw_211\n"
    "s_sub_u32 s63, s63, 1\n"
    ".Ld_212:\n"
    "s_branch .Lb_188\n"
    ".Ln_191:\n"
    ".Lw_213:\n"
    "ds_read_b64 v[116:117], v119 offset:160\n"
    "s_waitcnt lgkmcnt(0)\n"
    "v_min_u32 v117, v116, v117\n"
    "s_nop 0\n"
    "v_readfirstlane_b32 s38, v117\n"
    "s_cmp_ge_u32 s38, 38\n"
    "s_cbranch_scc1 .Ld_214\n"
    "s_sleep 1\n"
    "s_add_u32 s63, s63, 1\n"
    "s_bitcmp1_b32 s63, 19\n"
    "s_cbranch_scc0 .Lw_213\n"
    "s_sub_u32 s63, s63, 1\n"
    ".Ld_214:\n"
    "s_branch .Lb_192\n"
    ".Lfallback:\n"
    "v_mbcnt_lo_u32_b32 v116, -1, 0\n"
    "v_mbcnt_hi_u32_b32 v116, -1, v116\n"
    "v_lshlrev_b32 v116, 4, v116\n"
    "v_mov_b32 v120, 0xff800000\n"
    "s_lshl_b32 s40, s52, 17\n"
    "s_add_u32 s40, s40, 2097152\n"
    "s_add_u32 s46, s10, s40\n"
    "s_addc_u32 s47, s11, 0\n"
    "s_mov_b64 s[42:43], s[46:47]\n"
    "s_mov_b32 s36, 0\n"
    ".Lfb_1:\n"
    "global_load_dwordx4 v[104:107], v116, s[42:43] sc1\n"
    "s_add_u32 s42, s42, 0x800\n"
    "s_addc_u32 s43, s43, 0\n"
    "s_add_u32 s36, s36, 1\n"
    "s_waitcnt vmcnt(0)\n"
    "v_mfma_f32_32x32x16_bf16 v[32:47], v[104:107], v[96:99], 0\n"
    "s_nop 11\n"
    "v_max3_f32 v120, v120, v32, v33\n"
    "v_max3_f32 v120, v120, v34, v35\n"
    "v_max3_f32 v120, v120, v36, v37\n"
    "v_max3_f32 v120, v120, v38, v39\n"
    "v_max3_f32 v120, v120, v40, v41\n"
    "v_max3_f32 v120, v120, v42, v43\n"
    "v_max3_f32 v120, v120, v44, v45\n"
    "v_max3_f32 v120, v120, v46, v47\n"
    "s_cmp_lt_u32 s36, 64\n"
    "s_cbranch_scc1 .Lfb_1\n"
    "v_mov_b32 v117, v120\n"
    "s_nop 1\n"
    "v_permlane32_swap_b32 v120, v117\n"
    "v_max_f32 v120, v120, v117\n"
    "v_sub_f32 v64, 0, v120\n"
    "v_sub_f32 v65, 0, v120\n"
    "v_sub_f32 v66, 0, v120\n"
    "v_sub_f32 v67, 0, v120\n"
    "v_sub_f32 v68, 0, v120\n"
    "v_sub_f32 v69, 0, v120\n"
    "v_sub_f32 v70, 0, v120\n"
    "v_sub_f32 v71, 0, v120\n"
    "v_sub_f32 v72, 0, v120\n"
    "v_sub_f32 v73, 0, v120\n"
    "v_sub_f32 v74, 0, v120\n"
    "v_sub_f32 v75, 0, v120\n"
    "v_sub_f32 v76, 0, v120\n"
    "v_sub_f32 v77, 0, v120\n"
    "v_sub_f32 v78, 0, v120\n"
    "v_sub_f32 v79, 0, v120\n"
    "v_mov_b64 v[0:1], 0\n"
    "v_mov_b64 v[2:3], 0\n"
    "v_mov_b64 v[4:5], 0\n"
    "v_mov_b64 v[6:7], 0\n"
    "v_mov_b64 v[8:9], 0\n"
    "v_mov_b64 v[10:11], 0\n"
    "v_mov_b64 v[12:13], 0\n"
    "v_mov_b64 v[14:15], 0\n"
    "s_mov_b32 s54, 0\n"
    "s_mov_b32 s36, 0\n"
    ".Lfb_2:\n"
    "global_load_dwordx4 v[104:107], v116, s[46:47] sc1\n"
    "s_mov_b64 exec, s[56:57]\n"
    "global_load_dwordx4 v[108:111], v116, s[46:47] offset:1024 sc1\n"
    "global_load_dwordx4 v[112:115], v116, s[46:47] offset:1280 sc1\n"
    "s_mov_b64 exec, -1\n"
    "s_add_u32 s46, s46, 0x800\n"
    "s_addc_u32 s47, s47, 0\n"
    "s_add_u32 s36, s36, 1\n"
    "s_waitcnt vmcnt(0)\n"
    "v_mfma_f32_32x32x16_bf16 v[32:47], v[104:107], v[96:99], v[64:79]\n"
    "s_nop 11\n"
    "v_exp_f32 v32, v32\n"
    "v_exp_f32 v33, v33\n"
    "v_exp_f32 v34, v34\n"
    "v_exp_f32 v35, v35\n"
    "v_exp_f32 v36, v36\n"
    "v_exp_f32 v37, v37\n"
    "v_exp_f32 v38, v38\n"
    "v_exp_f32 v39, v39\n"
    "v_exp_f32 v40, v40\n"
    "v_exp_f32 v41, v41\n"
    "v_exp_f32 v42, v42\n"
    "v_exp_f32 v43, v43\n"
    "v_exp_f32 v44, v44\n"
    "v_exp_f32 v45, v45\n"
    "v_exp_f32 v46, v46\n"
    "v_exp_f32 v47, v47\n"
    "s_nop 1\n"
    "v_smfmac_f32_32x32x32_bf16 v[0:15], v[108:111], v[32:39], v118\n"
    "v_smfmac_f32_32x32x32_bf16 v[0:15], v[112:115], v[40:47], v118\n"
    "s_cmp_lt_u32 s36, 64\n"
    "s_cbranch_scc1 .Lfb_2\n"
    "s_nop 11\n"
    "s_branch .Lchk\n"
    ".Lend:\n"
    : "+v"(tid) : [ka] "s"(__builtin_amdgcn_kernarg_segment_ptr()), [bid] "s"(blockIdx.x)
    : "v1", "v2", "v3", "v4", "v5", "v6", "v7", "v8", "v9", "v10", "v11", "v12", "v13", "v14", "v15", "v16", "v17", "v18", "v19", "v20", "v21", "v22", "v23", "v24", "v25", "v26", "v27", "v28", "v29", "v30", "v31", "v32", "v33", "v34", "v35", "v36", "v37", "v38", "v39", "v40", "v41", "v42", "v43", "v44", "v45", "v46", "v47", "v48", "v49", "v50", "v51", "v52", "v53", "v54", "v55", "v56", "v57", "v58", "v59", "v60", "v61", "v62", "v63", "v64", "v65", "v66", "v67", "v68", "v69", "v70", "v71", "v72", "v73", "v74", "v75", "v76", "v77", "v78", "v79", "v80", "v81", "v82", "v83", "v84", "v85", "v86", "v87", "v88", "v89", "v90", "v91", "v92", "v93", "v94", "v95", "v96", "v97", "v98", "v99", "v100", "v101", "v102", "v103", "v104", "v105", "v106", "v107", "v108", "v109", "v110", "v111", "v112", "v113", "v114", "v115", "v116", "v117", "v118", "v119", "v120", "v121", "v122", "v123", "v124", "v125", "v126", "v127", "s4", "s5", "s6", "s7", "s8", "s9", "s10", "s11", "s12", "s13", "s14", "s15", "s16", "s17", "s18", "s19", "s20", "s21", "s22", "s23", "s24", "s25", "s26", "s27", "s28", "s29", "s30", "s31", "s33", "s34", "s35", "s36", "s37", "s38", "s39", "s40", "s41", "s42", "s43", "s44", "s45", "s46", "s47", "s48", "s49", "s50", "s51", "s52", "s53", "s54", "s55", "s56", "s57", "s58", "s59", "s60", "s61", "s62", "s63", "vcc", "scc", "memory");
}

extern "C" void kernel_launch(void* const* d_in, const int* in_sizes, int n_in, void* d_out, int out_size, void* d_ws, size_t ws_size, hipStream_t stream) {
  (void)hipFuncSetAttribute((const void*)k_fused, hipFuncAttributeMaxDynamicSharedMemorySize, 158976);
  k_fused<<<256, 1024, 158976, stream>>>(d_in[0], d_in[1], d_in[4], (char*)d_ws + (size_t)0 * (1u << 20), d_in[2], d_in[7], d_in[5], d_in[6], d_out);
}
```
